```python
import torch

def _softmax(x, axis=-1):
    return torch.softmax(x, dim=axis)
def _gelu(x):
    return torch.nn.functional.gelu(x, approximate="tanh")
def _silu(x):
    return torch.nn.functional.silu(x)
def _sigmoid(x):
    return torch.sigmoid(x)
def _layernorm(x, axis=-1, eps=1e-5):
    m = x.mean(dim=axis, keepdim=True)
    v = x.var(dim=axis, keepdim=True, unbiased=False)
    return (x - m) / torch.sqrt(v + eps)
def _scatter_add(tgt, idx, src, axis=0):
    return tgt.clone().index_add_(axis, idx, src)
def _segment_sum(src, seg, n):
    out = torch.zeros((n,) + src.shape[1:], dtype=src.dtype)
    return out.index_add_(0, seg, src)
def _pad(x, pw):
    flat = tuple(v for pair in reversed(pw) for v in pair)
    return torch.nn.functional.pad(x, flat)

kernel_name = 'sequential_preLN__sliding_block_cached__mixer_token_mlp__decode'

def setup_inputs():

    _w = (lambda a: (lambda t: t.to(torch.bfloat16) if t.is_floating_point() else t)(torch.as_tensor(a)))
    def _A_setup():
        B, Sq, D = 4, 1, 512
        Hq, Hkv, Sk, Dh = 32, 8, 8192, 128
        assert Hq % Hkv == 0, (Hq, Hkv)
        return {
            "x": _w((lambda *s: torch.randn(*s, dtype=torch.bfloat16) * 0.05)(B, Sq, D)),
            "k_cache": _w((lambda *s: torch.randn(*s, dtype=torch.bfloat16) * 3.2)(B, Hkv, Sk, Dh)),
            "v_cache": _w((lambda *s: torch.randn(*s, dtype=torch.bfloat16) * 0.8)(B, Hkv, Sk, Dh)),
            "wq": _w((lambda *s: torch.randn(*s, dtype=torch.bfloat16) * 3.2)(D, Hq * Dh) * (D ** -0.5)),
            "wo": _w((lambda *s: torch.randn(*s, dtype=torch.bfloat16) * 0.8)(Hq * Dh, D) * ((Hq * Dh) ** -0.5)),
        }
    _A_in = _A_setup()

    def _B_setup():
        B, S, D, F = 4, 1, 512, 1792
        return {
            "x": _w((lambda *s: torch.randn(*s, dtype=torch.bfloat16) * 0.05)(B, S, D)),
            "w1": _w((lambda *s: torch.randn(*s, dtype=torch.bfloat16) * 1.6)(S, F) * (S ** -0.5)),
            "b1": _w((lambda *s: torch.randn(*s, dtype=torch.bfloat16) * 1.6)(F) * 0.02),
            "w2": _w((lambda *s: torch.randn(*s, dtype=torch.bfloat16) * 1.6)(F, S) * (F ** -0.5)),
            "b2": _w((lambda *s: torch.randn(*s, dtype=torch.bfloat16) * 0.8)(S) * 0.02),
        }
    _B_in = _B_setup()

    return {
        "A_x": _A_in["x"],
        "A_k_cache": _A_in["k_cache"],
        "A_v_cache": _A_in["v_cache"],
        "A_wq": _A_in["wq"],
        "A_wo": _A_in["wo"],
        "B_x": _B_in["x"],
        "B_w1": _B_in["w1"],
        "B_b1": _B_in["b1"],
        "B_w2": _B_in["w2"],
        "B_b2": _B_in["b2"],
    }

def reference(A_x, A_k_cache, A_v_cache, A_wq, A_wo, B_x, B_w1, B_b1, B_w2, B_b2):
    def _A_ref(x, k_cache, v_cache, wq, wo):
        B, Sq, D = x.shape
        Hq, Dh = 32, 128
        Hkv, Sk = k_cache.shape[1], k_cache.shape[2]
        W = min(4096, max(256, Sk // 4))
        rep = Hq // Hkv
        q = (lambda x, *d: x.permute(*d))((x @ wq).reshape(B, Sq, Hq, Dh), 0, 2, 1, 3)
        k = (lambda x, n, axis: torch.repeat_interleave(x, n, dim=axis))(k_cache[:, :, Sk - W:, :], rep, axis=1)
        v = (lambda x, n, axis: torch.repeat_interleave(x, n, dim=axis))(v_cache[:, :, Sk - W:, :], rep, axis=1)
        scores = torch.einsum("bhqd,bhkd->bhqk", q, k) * (Dh ** -0.5)
        p = _softmax(scores, axis=-1)
        o = torch.einsum("bhqk,bhkd->bhqd", p, v)
        return (lambda x, *d: x.permute(*d))(o, 0, 2, 1, 3).reshape(B, Sq, Hq * Dh) @ wo

    def _B_ref(x, w1, b1, w2, b2):
        B, S, D = x.shape
        h = _layernorm(x, axis=-1)
        u = torch.einsum("bsd,sf->bdf", h, w1) + b1
        u = _gelu(u)
        y = torch.einsum("bdf,fs->bds", u, w2) + b2
        return (lambda x, *d: x.permute(*d))(y, 0, 2, 1)

    x = A_x
    h = x + _A_ref(_layernorm(x, axis=-1), A_k_cache, A_v_cache, A_wq, A_wo)
    h2 = h + _B_ref(_layernorm(h, axis=-1), B_w1, B_b1, B_w2, B_b2)
    return h2
```

```cpp
#include <hip/hip_runtime.h>
#include <hip/hip_bf16.h>
#include <stdint.h>

typedef unsigned short u16;
typedef __attribute__((ext_vector_type(4))) unsigned u32x4;

__device__ __forceinline__ float bf2f(u16 v) { return __uint_as_float(((unsigned)v) << 16); }
__device__ __forceinline__ float rbf(float f) {
    unsigned u = __float_as_uint(f);
    u += 0x7FFFu + ((u >> 16) & 1u);
    return __uint_as_float(u & 0xFFFF0000u);
}
__device__ __forceinline__ u16 f2bf(float f) { return (u16)(__float_as_uint(rbf(f)) >> 16); }

constexpr int B = 4, D = 512, HQ = 32, HKV = 8, SK = 8192, DH = 128, W = 2048, F = 1792;
constexpr int NSPLIT = 8, KPS = W / NSPLIT;

__device__ float block_sum(float v, float* red) {
    for (int o = 32; o > 0; o >>= 1) v += __shfl_xor(v, o);
    int w = threadIdx.x >> 6;
    __syncthreads();
    if ((threadIdx.x & 63) == 0) red[w] = v;
    __syncthreads();
    return red[0] + red[1] + red[2] + red[3];
}

__device__ void ln2(float& a, float& b, float* red) {
    float s = block_sum(a + b, red);
    float mean = s * (1.0f / D);
    float da = a - mean, db = b - mean;
    float ss = block_sum(da * da + db * db, red);
    float var = rbf(ss * (1.0f / D));
    float m = rbf(mean);
    float den = rbf(sqrtf(rbf(var + 1e-5f)));
    a = rbf(rbf(a - m) / den);
    b = rbf(rbf(b - m) / den);
}

__global__ void k_qproj(const u16* x, const u16* wq, float* q) {
    __shared__ float xn[B][D];
    __shared__ float red[4];
    __shared__ float part[4][B][64];
    int t = threadIdx.x;
    for (int b = 0; b < B; b++) {
        float a0 = bf2f(x[b * D + t]), a1 = bf2f(x[b * D + 256 + t]);
        ln2(a0, a1, red);
        xn[b][t] = a0; xn[b][256 + t] = a1;
    }
    __syncthreads();
    int col = blockIdx.x * 64 + (t & 63), kq = t >> 6;
    float acc[B] = {0, 0, 0, 0};
    for (int k = kq * 128; k < kq * 128 + 128; k++) {
        float w = bf2f(wq[(size_t)k * 4096 + col]);
        for (int b = 0; b < B; b++) acc[b] += xn[b][k] * w;
    }
    for (int b = 0; b < B; b++) part[kq][b][t & 63] = acc[b];
    __syncthreads();
    {
        int b = t >> 6, c = t & 63;
        float s = part[0][b][c] + part[1][b][c] + part[2][b][c] + part[3][b][c];
        q[b * 4096 + blockIdx.x * 64 + c] = rbf(s);
    }
}

__global__ void __launch_bounds__(256) k_attn(const float* q, const u16* kc, const u16* vc, float* po, float* pml) {
    __shared__ float qs[4][DH];
    __shared__ float p[4][KPS];
    __shared__ float red[4];
    int s = blockIdx.x, kvh = blockIdx.y, b = blockIdx.z, t = threadIdx.x;
    for (int i = t; i < 4 * DH; i += 256) qs[i / DH][i % DH] = q[b * 4096 + kvh * 512 + i];
    __syncthreads();
    size_t base = ((size_t)(b * HKV + kvh) * SK + (SK - W) + s * KPS) * DH;
    const u16* kr = kc + base + (size_t)t * DH;
    float sc[4] = {0, 0, 0, 0};
#pragma unroll 1
    for (int d = 0; d < DH; d += 8) {
        u32x4 v = *(const u32x4*)(kr + d);
        for (int j = 0; j < 4; j++) {
            float lo = __uint_as_float(v[j] << 16), hi = __uint_as_float(v[j] & 0xFFFF0000u);
            for (int h = 0; h < 4; h++) sc[h] += lo * qs[h][d + 2 * j] + hi * qs[h][d + 2 * j + 1];
        }
    }
    for (int h = 0; h < 4; h++) {
        float v = rbf(rbf(sc[h]) * 0.08838834764831845f);
        float m = v;
        for (int o = 32; o > 0; o >>= 1) m = fmaxf(m, __shfl_xor(m, o));
        __syncthreads();
        if ((t & 63) == 0) red[t >> 6] = m;
        __syncthreads();
        m = fmaxf(fmaxf(red[0], red[1]), fmaxf(red[2], red[3]));
        float e = __expf(v - m);
        p[h][t] = e;
        float l = block_sum(e, red);
        if (t == 0) {
            int slot = ((b * HQ + kvh * 4 + h) * NSPLIT + s);
            pml[slot * 2] = m; pml[slot * 2 + 1] = l;
        }
    }
    __syncthreads();
    int d = t & 127, hh = t >> 7;
    float a0 = 0, a1 = 0;
    for (int k = 0; k < KPS; k++) {
        float v = bf2f(vc[base + (size_t)k * DH + d]);
        a0 += p[2 * hh][k] * v; a1 += p[2 * hh + 1][k] * v;
    }
    po[((size_t)(b * HQ + kvh * 4 + 2 * hh) * NSPLIT + s) * DH + d] = a0;
    po[((size_t)(b * HQ + kvh * 4 + 2 * hh + 1) * NSPLIT + s) * DH + d] = a1;
}

__global__ void k_wo(const float* po, const float* pml, const u16* wo, float* part) {
    __shared__ float o[B][DH];
    int h = blockIdx.x, t = threadIdx.x;
    for (int i = t; i < B * DH; i += 256) {
        int b = i / DH, d = i % DH;
        int slot = (b * HQ + h) * NSPLIT;
        float M = -INFINITY;
        for (int s = 0; s < NSPLIT; s++) M = fmaxf(M, pml[(slot + s) * 2]);
        float L = 0, acc = 0;
        for (int s = 0; s < NSPLIT; s++) {
            float w = __expf(pml[(slot + s) * 2] - M);
            L += w * pml[(slot + s) * 2 + 1];
            acc += w * po[(size_t)(slot + s) * DH + d];
        }
        o[b][d] = rbf(acc / L);
    }
    __syncthreads();
    float acc[B][2] = {};
    for (int d = 0; d < DH; d++) {
        unsigned w = *(const unsigned*)(wo + (size_t)(h * DH + d) * D + 2 * t);
        float w0 = __uint_as_float(w << 16), w1 = __uint_as_float(w & 0xFFFF0000u);
        for (int b = 0; b < B; b++) { acc[b][0] += o[b][d] * w0; acc[b][1] += o[b][d] * w1; }
    }
    for (int b = 0; b < B; b++) {
        part[((size_t)h * B + b) * D + 2 * t] = acc[b][0];
        part[((size_t)h * B + b) * D + 2 * t + 1] = acc[b][1];
    }
}

__global__ void k_mlp(const u16* x, const float* part, const u16* w1, const u16* b1, const u16* w2, const u16* b2, u16* out) {
    __shared__ float red[4];
    __shared__ float hs[D], ts[D];
    int b = blockIdx.x >> 6, c0 = (blockIdx.x & 63) * 8, t = threadIdx.x;
    float a0 = 0, a1 = 0;
    for (int h = 0; h < HQ; h++) {
        a0 += part[((size_t)h * B + b) * D + t];
        a1 += part[((size_t)h * B + b) * D + 256 + t];
    }
    a0 = rbf(bf2f(x[b * D + t]) + rbf(a0));
    a1 = rbf(bf2f(x[b * D + 256 + t]) + rbf(a1));
    hs[t] = a0; hs[256 + t] = a1;
    ln2(a0, a1, red);
    ln2(a0, a1, red);
    ts[t] = a0; ts[256 + t] = a1;
    __syncthreads();
    for (int j = 0; j < 8; j++) {
        float tv = ts[c0 + j];
        float acc = 0;
        for (int f = t; f < F; f += 256) {
            float u = tv * bf2f(w1[f]) + bf2f(b1[f]);
            float z = 0.7978845608028654f * (u + 0.044715f * u * u * u);
            float g = u / (1.0f + __expf(-2.0f * z));
            acc += g * bf2f(w2[f]);
        }
        float y = block_sum(acc, red);
        if (t == 0) {
            float yy = rbf(rbf(y) + bf2f(b2[0]));
            out[b * D + c0 + j] = f2bf(hs[c0 + j] + yy);
        }
    }
}

extern "C" void kernel_launch(void* const* d_in, const int* in_sizes, int n_in,
                              void* d_out, int out_size, void* d_ws, size_t ws_size,
                              hipStream_t stream) {
    const u16* x = (const u16*)d_in[0];
    const u16* kc = (const u16*)d_in[1];
    const u16* vc = (const u16*)d_in[2];
    const u16* wq = (const u16*)d_in[3];
    const u16* wo = (const u16*)d_in[4];
    const u16* w1 = (const u16*)d_in[6];
    const u16* b1 = (const u16*)d_in[7];
    const u16* w2 = (const u16*)d_in[8];
    const u16* b2 = (const u16*)d_in[9];
    float* ws = (float*)d_ws;
    float* q = ws;
    float* pml = q + 16384;
    float* po = pml + 4096;
    float* part = po + 131072;
    k_qproj<<<64, 256, 0, stream>>>(x, wq, q);
    k_attn<<<dim3(NSPLIT, HKV, B), 256, 0, stream>>>(q, kc, vc, po, pml);
    k_wo<<<32, 256, 0, stream>>>(po, pml, wo, part);
    k_mlp<<<256, 256, 0, stream>>>(x, part, w1, b1, w2, b2, (u16*)d_out);
}
```

```cpp
#include <hip/hip_runtime.h>
#include <stdint.h>

typedef unsigned short u16;
typedef unsigned int u32;
typedef unsigned long long u64;
typedef __attribute__((ext_vector_type(4))) u32 u32x4;
typedef __attribute__((ext_vector_type(2))) u32 u32x2;
typedef __attribute__((ext_vector_type(4))) float f32x4;
typedef __attribute__((ext_vector_type(8))) short bf16x8;
typedef __attribute__((ext_vector_type(4))) short s16x4;
typedef __attribute__((ext_vector_type(2))) __bf16 bf16x2;
#define LAS __attribute__((address_space(3)))

#define WIN 8

constexpr int D = 512, SK = 8192, DH = 128, W = 2048, F = 1792;
constexpr float EPS = 1e-5f;
constexpr float LOG2E = 1.4426950408889634f;
constexpr u32 SPIN_CAP = 1u << 18;

constexpr int WS_EPOCH = 0;
constexpr int WS_Q   = 4096;
constexpr int WS_LSE = WS_Q + 65536;
constexpr int WS_O   = WS_LSE + 8192;
constexpr int WS_P   = WS_O + 524288;
constexpr int WS_H   = WS_P + 1048576;
constexpr int WS_END = WS_H + 524288;
constexpr size_t WS_STAMP = 64u << 20;

constexpr int L_WQ   = 0;
constexpr int L_XN   = 16384;
constexpr int L_Q    = L_XN + 4096;
constexpr int L_MAX  = L_Q + 1024;
constexpr int L_SUM  = L_MAX + 128;
constexpr int L_TAG  = L_SUM + 128;
constexpr int L_PART = L_TAG + 16;
constexpr int L_PAR  = L_PART + 8 * 8 * 17 * 16;
constexpr int L_END  = L_PAR + 3 * F * 4;
constexpr int LDS_BYTES = 84 * 1024;
static_assert(L_END <= LDS_BYTES, "lds");

__device__ __forceinline__ float bflo(u32 v) { return __uint_as_float(v << 16); }
__device__ __forceinline__ float bfhi(u32 v) { return __uint_as_float(v & 0xFFFF0000u); }
__device__ __forceinline__ u32 pack2(float lo, float hi) {
    bf16x2 v = {(__bf16)lo, (__bf16)hi};
    return __builtin_bit_cast(u32, v);
}
__device__ __forceinline__ float rbf(float x) { return (float)(__bf16)x; }
template <int CTRL> __device__ __forceinline__ float dpp(float x) {
    return __builtin_bit_cast(float, __builtin_amdgcn_mov_dpp(__builtin_bit_cast(int, x), CTRL, 0xf, 0xf, true));
}
__device__ __forceinline__ float x16_sum(float x) {
    auto s = __builtin_amdgcn_permlane16_swap(__float_as_uint(x), __float_as_uint(x), false, false);
    x = __uint_as_float(s[0]) + __uint_as_float(s[1]);
    auto t = __builtin_amdgcn_permlane32_swap(__float_as_uint(x), __float_as_uint(x), false, false);
    return __uint_as_float(t[0]) + __uint_as_float(t[1]);
}
__device__ __forceinline__ float x16_max(float x) {
    auto s = __builtin_amdgcn_permlane16_swap(__float_as_uint(x), __float_as_uint(x), false, false);
    x = fmaxf(__uint_as_float(s[0]), __uint_as_float(s[1]));
    auto t = __builtin_amdgcn_permlane32_swap(__float_as_uint(x), __float_as_uint(x), false, false);
    return fmaxf(__uint_as_float(t[0]), __uint_as_float(t[1]));
}
constexpr int XOR1 = 0xB1, XOR2 = 0x4E, ROR4 = 0x124, ROR8 = 0x128;
__device__ __forceinline__ float wave_sum(float x) {
    x += dpp<XOR1>(x); x += dpp<XOR2>(x); x += dpp<ROR4>(x); x += dpp<ROR8>(x);
    return x16_sum(x);
}
__device__ __forceinline__ f32x4 mfma16(u32x4 a, u32x4 b, f32x4 c) {
    return __builtin_amdgcn_mfma_f32_16x16x32_bf16(__builtin_bit_cast(bf16x8, a), __builtin_bit_cast(bf16x8, b), c, 0, 0, 0);
}
__device__ __forceinline__ u32x4 ldnt(const u16* p) { return __builtin_nontemporal_load((const u32x4*)p); }
#define SB() __builtin_amdgcn_sched_barrier(0)
#define BARRIER() do { asm volatile("s_waitcnt lgkmcnt(0)\n\ts_barrier" ::: "memory"); } while (0)
#define STR2(x) #x
#define STR(x) STR2(x)
constexpr int SC1 = 16;

template <int DD> __device__ __forceinline__ u32x4 colfrag(const u32x4 (&r)[8]) {
    constexpr u32 sel = (DD & 1) ? 0x07060302u : 0x05040100u;
    u32x4 f;
#pragma unroll
    for (int w = 0; w < 4; w++) f[w] = __builtin_amdgcn_perm(r[2 * w + 1][DD >> 1], r[2 * w][DD >> 1], sel);
    return f;
}

__device__ __forceinline__ void ln8(float (&v)[8]) {
    float s = ((v[0] + v[1]) + (v[2] + v[3])) + ((v[4] + v[5]) + (v[6] + v[7]));
    const float mean = wave_sum(s) * (1.0f / D);
    float ss = 0.f;
#pragma unroll
    for (int k = 0; k < 8; k++) { const float d = v[k] - mean; ss = fmaf(d, d, ss); }
    const float var = rbf(wave_sum(ss) * (1.0f / D));
    const float m = rbf(mean);
    const float den = rbf(__builtin_amdgcn_sqrtf(rbf(var + EPS)));
    const float inv = __builtin_amdgcn_rcpf(den);
#pragma unroll
    for (int k = 0; k < 8; k++) v[k] = rbf(rbf(v[k] - m) * inv);
}

#define STAMP(k) do {} while (0)

__global__ void __launch_bounds__(576, 3)
fused_kernel(const u16* __restrict__ x, const u16* __restrict__ kc, const u16* __restrict__ vc,
             const u16* __restrict__ wq, const u16* __restrict__ wo,
             const u16* __restrict__ w1, const u16* __restrict__ b1, const u16* __restrict__ w2,
             const u16* __restrict__ b2, u16* __restrict__ out, unsigned char* ws) {
    extern __shared__ __attribute__((aligned(16))) unsigned char smem[];
    LAS unsigned char* L = (LAS unsigned char*)smem;
    const int wg = blockIdx.x, kvh = wg & 7, r = wg >> 3, b = r >> 3, s = r & 7;
    const int lane = threadIdx.x & 63;
    const int wave = __builtin_amdgcn_readfirstlane(threadIdx.x >> 6);
    const int c = lane >> 4, n = lane & 15;
    const __amdgpu_buffer_rsrc_t rs = __builtin_amdgcn_make_buffer_rsrc((void*)ws, 0, WS_END, 0x00020000);
    STAMP(0);
    const int bj = wg >> 6, cc = wg & 63;

    if (wave == 0) {
        const u32 ep = __builtin_amdgcn_raw_buffer_load_b32(rs, WS_EPOCH, 0, SC1);
        u32x4 xr[4];
#pragma unroll
        for (int i = 0; i < 4; i++) xr[i] = *(const u32x4*)(x + i * D + lane * 8);
        SB();
        u32x4 wr[16];
        {
            const u16* p = wq + (size_t)(lane >> 1) * 4096 + kvh * 512 + r * 16 + (lane & 1) * 8;
#pragma unroll
            for (int i = 0; i < 16; i++) wr[i] = *(const u32x4*)(p + (size_t)i * 32 * 4096);
        }
        const u32x2 xres = *(const u32x2*)(x + bj * D + cc * 8 + (lane & 1) * 4);
        SB();
#pragma unroll
        for (int i = 0; i < 4; i++) {
            float v[8];
#pragma unroll
            for (int k = 0; k < 4; k++) { v[2 * k] = bflo(xr[i][k]); v[2 * k + 1] = bfhi(xr[i][k]); }
            ln8(v);
            u32x4 o;
#pragma unroll
            for (int k = 0; k < 4; k++) o[k] = pack2(v[2 * k], v[2 * k + 1]);
            *(LAS u32x4*)(L + L_XN + i * 1024 + lane * 16) = o;
        }
        const u32 tag = ep + 0x9E3779B9u;
        if (lane == 0) *(LAS u32*)(L + L_TAG) = tag;
        STAMP(1);
#pragma unroll
        for (int i = 0; i < 16; i++) *(LAS u32x4*)(L + L_WQ + i * 1024 + lane * 16) = wr[i];
        asm volatile("s_waitcnt lgkmcnt(0)" ::: "memory");
        STAMP(2);
        f32x4 qa = {0.f, 0.f, 0.f, 0.f};
        {
            LAS unsigned char* tb = L + L_WQ + (8 * c + (n >> 2)) * 32 + (n & 3) * 8;
            LAS unsigned char* ab = L + L_XN + (n & 3) * 1024 + c * 16;
#pragma unroll
            for (int ks = 0; ks < 16; ks++) {
                const s16x4 lo = __builtin_amdgcn_ds_read_tr16_b64_v4i16((LAS s16x4*)(tb + ks * 1024));
                const s16x4 hi = __builtin_amdgcn_ds_read_tr16_b64_v4i16((LAS s16x4*)(tb + ks * 1024 + 128));
                const u32x2 l2 = __builtin_bit_cast(u32x2, lo), h2 = __builtin_bit_cast(u32x2, hi);
                const u32x4 bf = {l2[0], l2[1], h2[0], h2[1]};
                const u32x4 af = *(LAS u32x4*)(ab + ks * 64);
                qa = mfma16(af, bf, qa);
            }
        }
        {
            const float qv = c == 0 ? qa[0] : c == 1 ? qa[1] : c == 2 ? qa[2] : qa[3];
            const float qn = dpp<XOR1>(qv);
            const u32x2 g = {pack2(qv, qn), tag};
            if ((n & 1) == 0)
                __builtin_amdgcn_raw_buffer_store_b64(g, rs, WS_Q + (kvh * 4 + c) * 2048 + r * 64 + n * 4, 0, SC1);
        }
        STAMP(3);
        {
            const int off = WS_Q + (kvh * 4 + b) * 2048 + lane * 16;
            u32x4 g0, g1;
            for (u32 spin = 0; spin < SPIN_CAP; spin++) {
                asm volatile("" ::: "memory");
                g0 = __builtin_amdgcn_raw_buffer_load_b128(rs, off, 0, SC1);
                g1 = __builtin_amdgcn_raw_buffer_load_b128(rs, off + 1024, 0, SC1);
                const bool ok = g0[1] == tag && g0[3] == tag && g1[1] == tag && g1[3] == tag;
                if (__all(ok)) break;
            }
            *(LAS u32x2*)(L + L_Q + lane * 8) = u32x2{g0[0], g0[2]};
            *(LAS u32x2*)(L + L_Q + 512 + lane * 8) = u32x2{g1[0], g1[2]};
        }
        STAMP(4);
        BARRIER();
        const int head = kvh * 4 + (r >> 3), q4 = (r >> 1) & 3, kap = r & 1;
        u32x4 wo0[8], wo1[8];
        {
            const u16* p = wo + (size_t)(head * 128 + q4 * 32 + 2 * c) * D + kap * 256 + n * 8;
#pragma unroll
            for (int j = 0; j < 8; j++) {
                wo0[j] = ldnt(p + (8 * (j & 3) + (j >> 2)) * D);
                wo1[j] = ldnt(p + (8 * (j & 3) + (j >> 2)) * D + 128);
            }
        }
        SB();
        BARRIER();
        BARRIER();
        STAMP(5);
        u32x4 f0[8], f1[8];
        f0[0] = colfrag<0>(wo0); f0[1] = colfrag<1>(wo0); f0[2] = colfrag<2>(wo0); f0[3] = colfrag<3>(wo0);
        f0[4] = colfrag<4>(wo0); f0[5] = colfrag<5>(wo0); f0[6] = colfrag<6>(wo0); f0[7] = colfrag<7>(wo0);
        f1[0] = colfrag<0>(wo1); f1[1] = colfrag<1>(wo1); f1[2] = colfrag<2>(wo1); f1[3] = colfrag<3>(wo1);
        f1[4] = colfrag<4>(wo1); f1[5] = colfrag<5>(wo1); f1[6] = colfrag<6>(wo1); f1[7] = colfrag<7>(wo1);
        SB();
        STAMP(6);
        const int bb = n >> 2, g = n & 3;
        u32x4 a0, a1, a2, a3, le;
        {
            const int off = WS_O + (((bb * 32 + head) * 8 + 2 * g) * 4 + q4) * 128 + c * 32;
            const int loff = WS_LSE + ((head * 4 + bb) * 8 + 2 * g) * 8;
            for (u32 spin = 0; spin < SPIN_CAP; spin++) {
                asm volatile("" ::: "memory");
                a0 = __builtin_amdgcn_raw_buffer_load_b128(rs, off, 0, SC1);
                a1 = __builtin_amdgcn_raw_buffer_load_b128(rs, off + 16, 0, SC1);
                a2 = __builtin_amdgcn_raw_buffer_load_b128(rs, off + 512, 0, SC1);
                a3 = __builtin_amdgcn_raw_buffer_load_b128(rs, off + 528, 0, SC1);
                le = __builtin_amdgcn_raw_buffer_load_b128(rs, loff, 0, SC1);
                const bool ok = a0[1] == tag && a0[3] == tag && a1[1] == tag && a1[3] == tag && a2[1] == tag && a2[3] == tag &&
                                a3[1] == tag && a3[3] == tag && le[1] == tag && le[3] == tag;
                if (__all(ok)) break;
            }
        }
        STAMP(7);
        u32x4 af;
        {
            const float l0 = __uint_as_float(le[0]), l1 = __uint_as_float(le[2]);
            float M = fmaxf(l0, l1);
            M = fmaxf(M, dpp<XOR1>(M)); M = fmaxf(M, dpp<XOR2>(M));
            const float e0 = __builtin_amdgcn_exp2f(l0 - M), e1 = __builtin_amdgcn_exp2f(l1 - M);
            float Ls = e0 + e1;
            Ls += dpp<XOR1>(Ls); Ls += dpp<XOR2>(Ls);
            const float inv = __builtin_amdgcn_rcpf(Ls);
            const float w0 = e0 * inv, w1_ = e1 * inv;
            af[0] = pack2(w0 * bflo(a0[0]) + w1_ * bflo(a2[0]), w0 * bfhi(a0[0]) + w1_ * bfhi(a2[0]));
            af[1] = pack2(w0 * bflo(a0[2]) + w1_ * bflo(a2[2]), w0 * bfhi(a0[2]) + w1_ * bfhi(a2[2]));
            af[2] = pack2(w0 * bflo(a1[0]) + w1_ * bflo(a3[0]), w0 * bfhi(a1[0]) + w1_ * bfhi(a3[0]));
            af[3] = pack2(w0 * bflo(a1[2]) + w1_ * bflo(a3[2]), w0 * bfhi(a1[2]) + w1_ * bfhi(a3[2]));
        }
        {
            const int rho = kvh * 16 + (r >> 1);
            const f32x4 z = {0.f, 0.f, 0.f, 0.f};
            float o0[8], o1[8];
#pragma unroll
            for (int d = 0; d < 8; d++) { const f32x4 t = mfma16(af, f0[d], z); o0[d] = (t[0] + t[1]) + (t[2] + t[3]); }
            const int off0 = WS_P + (c * 64 + kap * 32 + n) * 4096 + rho * 32;
            __builtin_amdgcn_raw_buffer_store_b128(u32x4{pack2(o0[0], o0[1]), tag, pack2(o0[2], o0[3]), tag}, rs, off0, 0, SC1);
            __builtin_amdgcn_raw_buffer_store_b128(u32x4{pack2(o0[4], o0[5]), tag, pack2(o0[6], o0[7]), tag}, rs, off0 + 16, 0, SC1);
#pragma unroll
            for (int d = 0; d < 8; d++) { const f32x4 t = mfma16(af, f1[d], z); o1[d] = (t[0] + t[1]) + (t[2] + t[3]); }
            __builtin_amdgcn_raw_buffer_store_b128(u32x4{pack2(o1[0], o1[1]), tag, pack2(o1[2], o1[3]), tag}, rs, off0 + 65536, 0, SC1);
            __builtin_amdgcn_raw_buffer_store_b128(u32x4{pack2(o1[4], o1[5]), tag, pack2(o1[6], o1[7]), tag}, rs, off0 + 65536 + 16, 0, SC1);
        }
        STAMP(8);
        {
            const int off = WS_P + wg * 4096 + lane * 16;
            for (u32 spin = 0; spin < SPIN_CAP; spin++) {
                asm volatile("" ::: "memory");
                a0 = __builtin_amdgcn_raw_buffer_load_b128(rs, off, 0, SC1);
                a1 = __builtin_amdgcn_raw_buffer_load_b128(rs, off + 1024, 0, SC1);
                a2 = __builtin_amdgcn_raw_buffer_load_b128(rs, off + 2048, 0, SC1);
                a3 = __builtin_amdgcn_raw_buffer_load_b128(rs, off + 3072, 0, SC1);
                const bool ok = a0[1] == tag && a0[3] == tag && a1[1] == tag && a1[3] == tag && a2[1] == tag && a2[3] == tag &&
                                a3[1] == tag && a3[3] == tag;
                if (__all(ok)) break;
            }
        }
        STAMP(9);
        {
            float v[4];
            v[0] = (bflo(a0[0]) + bflo(a1[0])) + (bflo(a2[0]) + bflo(a3[0]));
            v[1] = (bfhi(a0[0]) + bfhi(a1[0])) + (bfhi(a2[0]) + bfhi(a3[0]));
            v[2] = (bflo(a0[2]) + bflo(a1[2])) + (bflo(a2[2]) + bflo(a3[2]));
            v[3] = (bfhi(a0[2]) + bfhi(a1[2])) + (bfhi(a2[2]) + bfhi(a3[2]));
#pragma unroll
            for (int k = 0; k < 4; k++) {
                float t = v[k];
                t += dpp<XOR2>(t); t += dpp<ROR4>(t); t += dpp<ROR8>(t);
                v[k] = rbf(x16_sum(t));
            }
            const u32 h01 = pack2(bflo(xres[0]) + v[0], bfhi(xres[0]) + v[1]);
            const u32 h23 = pack2(bflo(xres[1]) + v[2], bfhi(xres[1]) + v[3]);
            const u32x4 hg = {h01, tag, h23, tag};
            const int off = WS_H + (bj * 64 + (lane >> 1)) * 2048 + cc * 32 + (lane & 1) * 16;
            __builtin_amdgcn_raw_buffer_store_b128(hg, rs, off, 0, SC1);
            __builtin_amdgcn_raw_buffer_store_b128(hg, rs, off + 32 * 2048, 0, SC1);
        }
        STAMP(10);
    } else {
        const int dw = wave - 1;
        u32x2 pw1, pb1, pw2;
        if (dw < 7) {
            pw1 = *(const u32x2*)(w1 + dw * 256 + lane * 4);
            pb1 = *(const u32x2*)(b1 + dw * 256 + lane * 4);
            pw2 = *(const u32x2*)(w2 + dw * 256 + lane * 4);
        }
        const u32 b2v = b2[0];
        SB();
        const size_t base = ((size_t)(b * 8 + kvh) * SK + (SK - W) + s * 256 + dw * 32) * DH;
        u32x4 kr[8], vr[8];
        {
            const u16* kp = kc + base + (4 * (n & 3) + (n >> 2)) * DH + 8 * c;
            const u16* vp = vc + base + lane * 8;
#pragma unroll
            for (int j = 0; j < 16; j++) {
                if (j >= WIN) { asm volatile("s_waitcnt vmcnt(%0)" :: "n"(WIN - 1) : "memory"); }
                if (j < 8) kr[j] = ldnt(kp + (j >> 2) * 16 * DH + (j & 3) * 32);
                else vr[j - 8] = ldnt(vp + (j - 8) * 512);
                SB();
            }
        }
        STAMP(1);
        if (dw < 7) {
            LAS unsigned char* pp = L + L_PAR + dw * 1024 + lane * 16;
            *(LAS f32x4*)(pp) = f32x4{bflo(pw1[0]), bfhi(pw1[0]), bflo(pw1[1]), bfhi(pw1[1])};
            *(LAS f32x4*)(pp + F * 4) = f32x4{bflo(pb1[0]), bfhi(pb1[0]), bflo(pb1[1]), bfhi(pb1[1])};
            *(LAS f32x4*)(pp + F * 8) = f32x4{bflo(pw2[0]), bfhi(pw2[0]), bflo(pw2[1]), bfhi(pw2[1])};
        }
        SB();
        BARRIER();
        STAMP(2);
        const u32 tag = *(LAS u32*)(L + L_TAG);
        float sc[8];
        {
            LAS unsigned char* qb = L + L_Q + (n & 3) * 256 + c * 16;
            f32x4 s0 = {0.f, 0.f, 0.f, 0.f}, s1 = {0.f, 0.f, 0.f, 0.f};
#pragma unroll
            for (int ks = 0; ks < 4; ks++) {
                const u32x4 qf = *(LAS u32x4*)(qb + ks * 64);
                s0 = mfma16(kr[ks], qf, s0);
                s1 = mfma16(kr[4 + ks], qf, s1);
            }
#pragma unroll
            for (int i = 0; i < 4; i++) {
                sc[i] = rbf(rbf(s0[i]) * 0.08838834764831845f);
                sc[4 + i] = rbf(rbf(s1[i]) * 0.08838834764831845f);
            }
        }
        float mx = fmaxf(fmaxf(fmaxf(sc[0], sc[1]), fmaxf(sc[2], sc[3])), fmaxf(fmaxf(sc[4], sc[5]), fmaxf(sc[6], sc[7])));
        mx = x16_max(mx);
        if (lane < 4) *(LAS float*)(L + L_MAX + lane * 32 + dw * 4) = mx;
        BARRIER();
        STAMP(3);
        float M;
        {
            const f32x4 m0 = *(LAS f32x4*)(L + L_MAX + (n & 3) * 32), m1 = *(LAS f32x4*)(L + L_MAX + (n & 3) * 32 + 16);
            M = fmaxf(fmaxf(fmaxf(m0[0], m0[1]), fmaxf(m0[2], m0[3])), fmaxf(fmaxf(m1[0], m1[1]), fmaxf(m1[2], m1[3])));
        }
        u32x4 pf;
        {
            float p[8];
#pragma unroll
            for (int i = 0; i < 8; i++) p[i] = __builtin_amdgcn_exp2f((sc[i] - M) * LOG2E);
#pragma unroll
            for (int i = 0; i < 4; i++) pf[i] = pack2(p[2 * i], p[2 * i + 1]);
            float ls = 0.f;
#pragma unroll
            for (int i = 0; i < 4; i++) ls += bflo(pf[i]) + bfhi(pf[i]);
            ls = x16_sum(ls);
            if (lane < 4) *(LAS float*)(L + L_SUM + lane * 32 + dw * 4) = ls;
        }
        SB();
        STAMP(4);
        {
            const f32x4 z = {0.f, 0.f, 0.f, 0.f};
            f32x4 o[8];
            o[0] = mfma16(colfrag<0>(vr), pf, z); o[1] = mfma16(colfrag<1>(vr), pf, z);
            o[2] = mfma16(colfrag<2>(vr), pf, z); o[3] = mfma16(colfrag<3>(vr), pf, z);
            o[4] = mfma16(colfrag<4>(vr), pf, z); o[5] = mfma16(colfrag<5>(vr), pf, z);
            o[6] = mfma16(colfrag<6>(vr), pf, z); o[7] = mfma16(colfrag<7>(vr), pf, z);
            if (n < 4) {
                LAS unsigned char* pb = L + L_PART + dw * (8 * 17 * 16) + (c * 4 + n) * 16;
#pragma unroll
                for (int d = 0; d < 8; d++) *(LAS f32x4*)(pb + d * (17 * 16)) = o[d];
            }
        }
        STAMP(5);
        BARRIER();
        STAMP(6);
        if (dw < 2) {
            const int h = dw * 2 + (lane >> 5), cq = (lane >> 3) & 3, d = lane & 7;
            LAS unsigned char* pb = L + L_PART + (d * 17 + cq * 4 + h) * 16;
            f32x4 acc = *(LAS f32x4*)(pb);
#pragma unroll
            for (int w = 1; w < 8; w++) acc += *(LAS f32x4*)(pb + w * (8 * 17 * 16));
            const f32x4 l0 = *(LAS f32x4*)(L + L_SUM + h * 32), l1 = *(LAS f32x4*)(L + L_SUM + h * 32 + 16);
            const float ls = ((l0[0] + l0[1]) + (l0[2] + l0[3])) + ((l1[0] + l1[1]) + (l1[2] + l1[3]));
            const float inv = __builtin_amdgcn_rcpf(ls);
            const u32x4 og = {pack2(acc[0] * inv, acc[1] * inv), tag, pack2(acc[2] * inv, acc[3] * inv), tag};
            __builtin_amdgcn_raw_buffer_store_b128(og, rs, WS_O + ((b * 32 + kvh * 4 + h) * 8 + s) * 512 + (lane & 31) * 16, 0, SC1);
        } else if (dw == 2) {
            const int h = n & 3;
            const f32x4 l0 = *(LAS f32x4*)(L + L_SUM + h * 32), l1 = *(LAS f32x4*)(L + L_SUM + h * 32 + 16);
            const float ls = ((l0[0] + l0[1]) + (l0[2] + l0[3])) + ((l1[0] + l1[1]) + (l1[2] + l1[3]));
            const float lse = M * LOG2E + __builtin_amdgcn_logf(ls);
            if (lane < 4)
                __builtin_amdgcn_raw_buffer_store_b64(u32x2{__float_as_uint(lse), tag}, rs, WS_LSE + (((kvh * 4 + h) * 4 + b) * 8 + s) * 8, 0, SC1);
        }
        STAMP(7);
        f32x4 cw1[7], cb1[7], cw2[7];
#pragma unroll
        for (int it = 0; it < 7; it++) {
            LAS unsigned char* pp = L + L_PAR + it * 1024 + lane * 16;
            cw1[it] = *(LAS f32x4*)(pp);
            cb1[it] = *(LAS f32x4*)(pp + F * 4);
            cw2[it] = *(LAS f32x4*)(pp + F * 8);
        }
        SB();
        u32x4 g0, g1;
        {
            const int off = WS_H + wg * 2048 + lane * 16;
            for (u32 spin = 0; spin < SPIN_CAP; spin++) {
                asm volatile("" ::: "memory");
                g0 = __builtin_amdgcn_raw_buffer_load_b128(rs, off, 0, SC1);
                g1 = __builtin_amdgcn_raw_buffer_load_b128(rs, off + 1024, 0, SC1);
                const bool ok = g0[1] == tag && g0[3] == tag && g1[1] == tag && g1[3] == tag;
                if (__all(ok)) break;
            }
        }
        STAMP(8);
        float v[8] = {bflo(g0[0]), bfhi(g0[0]), bflo(g0[2]), bfhi(g0[2]), bflo(g1[0]), bfhi(g1[0]), bflo(g1[2]), bfhi(g1[2])};
        const int ksel = (cc >> 5) * 4 + (dw & 3), lsel = 2 * (cc & 31) + (dw >> 2);
        float hsel = v[0];
#pragma unroll
        for (int k = 1; k < 8; k++) hsel = (ksel == k) ? v[k] : hsel;
        const float hown = __builtin_bit_cast(float, __builtin_amdgcn_readlane(__builtin_bit_cast(int, hsel), lsel));
        ln8(v);
        ln8(v);
        float tsel = v[0];
#pragma unroll
        for (int k = 1; k < 8; k++) tsel = (ksel == k) ? v[k] : tsel;
        const float t = __builtin_bit_cast(float, __builtin_amdgcn_readlane(__builtin_bit_cast(int, tsel), lsel));
        STAMP(9);
        constexpr float K0 = -2.0f * 0.7978845608028654f * LOG2E, K1 = K0 * 0.044715f;
        float acc0 = 0.f, acc1 = 0.f;
#pragma unroll
        for (int it = 0; it < 7; it++) {
#pragma unroll
            for (int k = 0; k < 4; k++) {
                const float u = fmaf(t, cw1[it][k], cb1[it][k]);
                const float z = u * fmaf(u * u, K1, K0);
                const float e = __builtin_amdgcn_exp2f(z);
                const float rc = __builtin_amdgcn_rcpf(1.0f + e);
                if (k & 1) acc1 = fmaf(u * rc, cw2[it][k], acc1);
                else acc0 = fmaf(u * rc, cw2[it][k], acc0);
            }
        }
        const float y = wave_sum(acc0 + acc1);
        const float yy = rbf(rbf(y) + bflo(b2v));
        const u32 res = pack2(hown + yy, 0.f);
        if (lane == 0) out[bj * D + cc * 8 + dw] = (u16)res;
        if (wg == 0 && dw == 0 && lane == 0) __builtin_amdgcn_raw_buffer_store_b32(tag, rs, WS_EPOCH, 0, SC1);
        STAMP(10);
    }
}

extern "C" void kernel_launch(void* const* d_in, const int* in_sizes, int n_in,
                              void* d_out, int out_size, void* d_ws, size_t ws_size,
                              hipStream_t stream) {
    (void)in_sizes; (void)n_in; (void)out_size; (void)ws_size;
    (void)hipFuncSetAttribute((const void*)fused_kernel, hipFuncAttributeMaxDynamicSharedMemorySize, LDS_BYTES);
    fused_kernel<<<256, 576, LDS_BYTES, stream>>>(
        (const u16*)d_in[0], (const u16*)d_in[1], (const u16*)d_in[2], (const u16*)d_in[3], (const u16*)d_in[4],
        (const u16*)d_in[6], (const u16*)d_in[7], (const u16*)d_in[8], (const u16*)d_in[9],
        (u16*)d_out, (unsigned char*)d_ws);
}
```

```cpp
#include <hip/hip_runtime.h>
#include <stdint.h>

typedef unsigned short u16;
typedef unsigned int u32;
typedef unsigned long long u64;
typedef __attribute__((ext_vector_type(4))) u32 u32x4;
typedef __attribute__((ext_vector_type(2))) u32 u32x2;
typedef __attribute__((ext_vector_type(4))) float f32x4;
typedef __attribute__((ext_vector_type(2))) float f32x2;
typedef __attribute__((ext_vector_type(8))) short bf16x8;
typedef __attribute__((ext_vector_type(4))) short s16x4;
typedef __attribute__((ext_vector_type(2))) __bf16 bf16x2;
#define LAS __attribute__((address_space(3)))
#define GAS __attribute__((address_space(1)))

#define WIN 7
#define DQ 8
#define DL 8
#define DA 3
#define DB 8
#define CODE_KB 7

constexpr int D = 512, SK = 8192, DH = 128, W = 2048, F = 1792;
constexpr float EPS = 1e-5f;
constexpr float LOG2E = 1.4426950408889634f;
constexpr u32 SPIN_CAP = 1u << 18;
constexpr u32 TEXP0 = 114;

constexpr int WS_EPOCH = 0;
constexpr int WS_Q   = 4096;
constexpr int WS_LSE = WS_Q + 65536;
constexpr int WS_TAB = WS_LSE + 8192;
constexpr int WS_P   = WS_TAB + 32768;
constexpr int WS_O   = WS_P + 131072;
constexpr int WS_END = WS_O + 524288;
constexpr size_t WS_STAMP = 64u << 20;
constexpr int OOB = 0x7ffffff0;

constexpr int L_WQ   = 0;
constexpr int L_XN   = 16384;
constexpr int L_X    = L_XN + 4096;
constexpr int L_Q    = L_X + 4096;
constexpr int L_FLG  = L_Q + 1024;
constexpr int L_MX   = L_FLG + 64;
constexpr int L_LS   = L_MX + 128;
constexpr int L_TP   = L_LS + 128;
constexpr int L_S2   = L_TP + 1024;
constexpr int L_QP   = L_S2 + 64;
constexpr int L_PP   = L_QP + 4096;
constexpr int L_R2   = L_PP + 4096;
constexpr int L_PART = L_R2 + 4096;
constexpr int L_WO   = L_PART + 16384;
constexpr int L_AT   = L_WO + 16384;
constexpr int L_TAB  = L_AT + 16384;
constexpr int L_PAR  = L_TAB + 8192;
constexpr int L_END  = L_PAR + 3 * F * 2;
constexpr int LDS_BYTES = 108 * 1024;
static_assert(L_END <= LDS_BYTES, "lds");

__device__ __forceinline__ float bflo(u32 v) { return __uint_as_float(v << 16); }
__device__ __forceinline__ float bfhi(u32 v) { return __uint_as_float(v & 0xFFFF0000u); }
__device__ __forceinline__ u32 pack2(float lo, float hi) {
    bf16x2 v = {(__bf16)lo, (__bf16)hi};
    return __builtin_bit_cast(u32, v);
}
__device__ __forceinline__ float rbf(float x) { return (float)(__bf16)x; }
template <int CTRL> __device__ __forceinline__ float dpp(float x) {
    return __builtin_bit_cast(float, __builtin_amdgcn_mov_dpp(__builtin_bit_cast(int, x), CTRL, 0xf, 0xf, true));
}
__device__ __forceinline__ float sw32(float a, float b) {
    auto t = __builtin_amdgcn_permlane32_swap(__float_as_uint(a), __float_as_uint(b), false, false);
    return __uint_as_float(t[0]) + __uint_as_float(t[1]);
}
__device__ __forceinline__ float sw16(float a, float b) {
    auto t = __builtin_amdgcn_permlane16_swap(__float_as_uint(a), __float_as_uint(b), false, false);
    return __uint_as_float(t[0]) + __uint_as_float(t[1]);
}
__device__ __forceinline__ float x16_sum(float x) { x = sw16(x, x); return sw32(x, x); }
__device__ __forceinline__ float x16_max(float x) {
    auto s = __builtin_amdgcn_permlane16_swap(__float_as_uint(x), __float_as_uint(x), false, false);
    x = fmaxf(__uint_as_float(s[0]), __uint_as_float(s[1]));
    auto t = __builtin_amdgcn_permlane32_swap(__float_as_uint(x), __float_as_uint(x), false, false);
    return fmaxf(__uint_as_float(t[0]), __uint_as_float(t[1]));
}
constexpr int XOR1 = 0xB1, XOR2 = 0x4E, ROR4 = 0x124, ROR8 = 0x128, HMIR = 0x141;
__device__ __forceinline__ float row_sum(float x) {
    x += dpp<XOR1>(x); x += dpp<XOR2>(x); x += dpp<ROR4>(x); x += dpp<ROR8>(x);
    return x;
}
__device__ __forceinline__ float wave_sum(float x) { return x16_sum(row_sum(x)); }
__device__ __forceinline__ f32x4 mfma16(u32x4 a, u32x4 b, f32x4 c) {
    return __builtin_amdgcn_mfma_f32_16x16x32_bf16(__builtin_bit_cast(bf16x8, a), __builtin_bit_cast(bf16x8, b), c, 0, 0, 0);
}
__device__ __forceinline__ u32x4 ldnt(const u16* p) { return __builtin_nontemporal_load((const u32x4*)p); }
__device__ __forceinline__ u32x4 trfrag(LAS unsigned char* lo, LAS unsigned char* hi) {
    const u32x2 l = __builtin_bit_cast(u32x2, __builtin_amdgcn_ds_read_tr16_b64_v4i16((LAS s16x4*)lo));
    const u32x2 h = __builtin_bit_cast(u32x2, __builtin_amdgcn_ds_read_tr16_b64_v4i16((LAS s16x4*)hi));
    return u32x4{l[0], l[1], h[0], h[1]};
}
#define SB() __builtin_amdgcn_sched_barrier(0)
#define MEMB() asm volatile("" ::: "memory")
#define BARRIER() do { asm volatile("s_waitcnt lgkmcnt(0)\n\ts_barrier" ::: "memory"); } while (0)
#define LGK0() asm volatile("s_waitcnt lgkmcnt(0)" ::: "memory")
constexpr int SC1 = 16, NT = 2;
#define LD16(off) __builtin_amdgcn_raw_buffer_load_b128(rs, off, 0, SC1)
#define ST8(v, off) __builtin_amdgcn_raw_buffer_store_b64(v, rs, off, 0, SC1)

#define G_LD(i, so) "s_mov_b64 exec, %[m" #i "]\n\tbuffer_load_dwordx4 %[a" #i "], %[vo], %[rs], " so " offen sc1\n\t"
#define G_END "s_mov_b64 exec, -1\n\ts_waitcnt vmcnt(0)"
template <int N, int NAP>
__device__ __forceinline__ void gather(u32x4 (&a)[N], __amdgpu_buffer_rsrc_t rs, int off, int stride, u32 tag, u64 act) {
    u64 m[N];
#pragma unroll
    for (int i = 0; i < N; i++) { m[i] = act; asm volatile("" : "=v"(a[i])); }
#pragma unroll 1
    for (u32 spin = 0; spin < SPIN_CAP; spin++) {
        if constexpr (N == 4)
            asm volatile(G_LD(0, "0") G_LD(1, "%[s1]") G_LD(2, "%[s2]") G_LD(3, "%[s3]") G_END
                : [a0] "+v"(a[0]), [a1] "+v"(a[1]), [a2] "+v"(a[2]), [a3] "+v"(a[3])
                : [m0] "s"(m[0]), [m1] "s"(m[1]), [m2] "s"(m[2]), [m3] "s"(m[3]), [vo] "v"(off), [rs] "s"(rs),
                  [s1] "s"(stride), [s2] "s"(2 * stride), [s3] "s"(3 * stride) : "memory");
        else if constexpr (N == 2)
            asm volatile(G_LD(0, "0") G_LD(1, "%[s1]") G_END
                : [a0] "+v"(a[0]), [a1] "+v"(a[1])
                : [m0] "s"(m[0]), [m1] "s"(m[1]), [vo] "v"(off), [rs] "s"(rs), [s1] "s"(stride) : "memory");
        else
            asm volatile(G_LD(0, "0") G_END : [a0] "+v"(a[0]) : [m0] "s"(m[0]), [vo] "v"(off), [rs] "s"(rs) : "memory");
        u64 any = 0;
#pragma unroll
        for (int i = 0; i < N; i++) { m[i] = __ballot(a[i][1] != tag || a[i][3] != tag) & act; any |= m[i]; }
        if (!any) break;
        if (NAP) __builtin_amdgcn_s_sleep(NAP);
    }
}

template <int DD> __device__ __forceinline__ u32x4 colfrag(const u32x4 (&r)[8]) {
    constexpr u32 sel = (DD & 1) ? 0x07060302u : 0x05040100u;
    u32x4 f;
#pragma unroll
    for (int w = 0; w < 4; w++) f[w] = __builtin_amdgcn_perm(r[2 * w + 1][DD >> 1], r[2 * w][DD >> 1], sel);
    return f;
}

constexpr float GK0 = -2.0f * 0.7978845608028654f * LOG2E, GK1 = GK0 * 0.044715f;
__device__ __forceinline__ f32x2 unit2(float t, f32x2 w1, f32x2 b1, f32x2 w2, f32x2 acc) {
    const f32x2 u = __builtin_elementwise_fma(f32x2{t, t}, w1, b1);
    const f32x2 z = u * __builtin_elementwise_fma(u * u, f32x2{GK1, GK1}, f32x2{GK0, GK0});
    const f32x2 e = f32x2{__builtin_amdgcn_exp2f(z[0]), __builtin_amdgcn_exp2f(z[1])} + f32x2{1.f, 1.f};
    const f32x2 r = {__builtin_amdgcn_rcpf(e[0]), __builtin_amdgcn_rcpf(e[1])};
    return __builtin_elementwise_fma(u * w2, r, acc);
}
__device__ __forceinline__ f32x2 un2(u32 v) { return f32x2{bflo(v), bfhi(v)}; }

#define STAMP(k) do {} while (0)
#define FIN() do {} while (0)

#define RSRC(p, nbytes) __builtin_amdgcn_make_buffer_rsrc((void*)(p), 0, nbytes, 0x00020000)
#define LDNT(r, v, s) __builtin_amdgcn_raw_buffer_load_b128(r, v, s, NT)
template <int K> __device__ __forceinline__ float qbc(float x) { return dpp<K * 0x55>(x); }

extern "C" __global__ void __launch_bounds__(1024, 4)
fused_kernel(const u16* __restrict__ kc, const u16* __restrict__ vc, unsigned char* ws, const u16* __restrict__ wq,
             const u16* __restrict__ x, const u16* __restrict__ wo,
             const u16* __restrict__ w1, const u16* __restrict__ b1, const u16* __restrict__ w2,
             const u16* __restrict__ b2, u16* out) {
    extern __shared__ __attribute__((aligned(16))) unsigned char smem[];
    LAS unsigned char* L = (LAS unsigned char*)smem;
    const int wg = blockIdx.x, kvh = wg & 7, r = wg >> 3, b = r >> 3, s = r & 7;
    const int lane = threadIdx.x & 63;
    const int wave = __builtin_amdgcn_readfirstlane(threadIdx.x >> 6);
    const int c = lane >> 4, n = lane & 15, dw = wave & 7;
    STAMP(0);
    asm volatile("" :: "s"(kc), "s"(vc), "s"(ws), "s"(wq), "s"(x), "s"(wo), "s"(w1), "s"(b1), "s"(w2), "s"(b2), "s"(out));
    u32 ep = *(const __attribute__((address_space(4))) u32*)(ws + WS_EPOCH);
    const __amdgpu_buffer_rsrc_t rs = RSRC(ws, WS_END);
    const bool cons = (wg & 0xF9) == 0;
    const int bj = wg >> 1;
    const int rho = r & 1, cg = r >> 1;
    const bool kw = wave < 8;

    u32x4 xr, e0, e1, rr[8];
    u32x2 pw1, pb1, pw2;
    u32 b2v;
    if (kw) {
        __builtin_amdgcn_s_setprio(1);
        xr = *(const u32x4*)(x + (dw & 3) * D + lane * 8);
    }
    {
        const size_t base = ((size_t)(b * 8 + kvh) * SK + (SK - W) + s * 256 + dw * 32) * DH;
        const __amdgpu_buffer_rsrc_t rkv = RSRC((kw ? kc : vc) + base, 8192);
        const int kvo = kw ? (n >> 2) * 256 + c * 64 + (n & 3) * 16 : lane * 16;
        const __amdgpu_buffer_rsrc_t rw = RSRC(kw ? wq + (size_t)dw * 64 * 4096 + kvh * 512 + r * 16
                                                  : wo + (size_t)(kvh * 512 + rho * 256 + dw * 32) * D + cg * 32, kw ? 63 * 8192 + 32 : 31 * 1024 + 64);
        const int sg = lane >> 2, cp = sg >> 3;
        const int wof = kw ? (lane >> 1) * 8192 + (lane & 1) * 16 : (8 * (sg & 3) + 2 * cp + ((sg >> 2) & 1)) * 1024 + ((lane & 3) ^ (2 * cp)) * 16;
        const int wst = kw ? 32 * 8192 : 4096;
        const __amdgpu_buffer_rsrc_t ra = kw ? rw : rkv, rb = kw ? rkv : rw;
        const int ao = kw ? wof : kvo, bo = kw ? kvo : wof, s0 = kw ? 0 : 2048;
        e0 = LDNT(ra, ao, 0); SB();
        e1 = LDNT(ra, ao, kw ? wst : 1024); SB();
        if (kw) {
            const int po = (dw ? dw - 1 : 0) * 256 + lane * 4;
            pw1 = *(const u32x2*)(w1 + po);
            pb1 = *(const u32x2*)(b1 + po);
            pw2 = *(const u32x2*)(w2 + po);
            b2v = b2[0];
            SB();
            __builtin_amdgcn_s_setprio(0);
        }
#pragma unroll
        for (int j = 0; j < 6; j++) { rr[j] = LDNT(rkv, kvo + (j & 3) * 1024, s0 + (j >> 2) * 4096); SB(); }
        rr[6] = LDNT(rb, bo, kw ? 6144 : 0); SB();
        rr[7] = LDNT(rb, bo, kw ? 7168 : wst); SB();
    }
    int wv2 = wave;
    asm volatile("" : "+s"(ep), "+s"(wv2));
    const u32 tag = ep + 0x9E3779B9u;
    STAMP(1);

    if (__builtin_expect(wv2 >= 8, 1)) {
        BARRIER();
        __builtin_amdgcn_s_setprio(2);
        STAMP(2);
        u32x4 pfm[4];
        {
            const int h = n & 3;
            const f32x4 m0 = *(LAS f32x4*)(L + L_MX + h * 32), m1 = *(LAS f32x4*)(L + L_MX + h * 32 + 16);
            const f32x4 l0 = *(LAS f32x4*)(L + L_LS + h * 32), l1 = *(LAS f32x4*)(L + L_LS + h * 32 + 16);
            const float mo = *(LAS float*)(L + L_MX + h * 32 + dw * 4);
            const f32x4 p0 = *(LAS f32x4*)(L + L_PP + dw * 512 + (c * 4 + h) * 32), p1 = *(LAS f32x4*)(L + L_PP + dw * 512 + (c * 4 + h) * 32 + 16);
            const float M = fmaxf(fmaxf(fmaxf(m0[0], m0[1]), fmaxf(m0[2], m0[3])), fmaxf(fmaxf(m1[0], m1[1]), fmaxf(m1[2], m1[3])));
            float ls = 0.f;
#pragma unroll
            for (int i = 0; i < 4; i++) {
                ls = fmaf(l0[i], __builtin_amdgcn_exp2f(m0[i] - M), ls);
                ls = fmaf(l1[i], __builtin_amdgcn_exp2f(m1[i] - M), ls);
            }
            const float sc = __builtin_amdgcn_exp2f(mo - M) * __builtin_amdgcn_rcpf(ls);
            const u32x4 pf = {pack2(p0[0] * sc, p0[1] * sc), pack2(p0[2] * sc, p0[3] * sc), pack2(p1[0] * sc, p1[1] * sc), pack2(p1[2] * sc, p1[3] * sc)};
            const u32x4 zz = {0u, 0u, 0u, 0u};
#pragma unroll
            for (int g = 0; g < 4; g++) pfm[g] = (n >> 2) == g ? pf : zz;
            const float lse = M + __builtin_amdgcn_logf(ls);
            ST8((u32x2{__float_as_uint(lse), tag}), (dw == 7 && lane < 4) ? WS_LSE + ((kvh * 4 + lane) * 32 + r) * 8 : OOB);
        }
        SB();
        STAMP(3);
        {
            const u32x4 vr[8] = {e0, e1, rr[0], rr[1], rr[2], rr[3], rr[4], rr[5]};
            const f32x4 z = {0.f, 0.f, 0.f, 0.f};
            f32x4 a0 = mfma16(colfrag<0>(vr), pfm[0], z), a1 = mfma16(colfrag<4>(vr), pfm[0], z);
            a0 = mfma16(colfrag<1>(vr), pfm[1], a0); a1 = mfma16(colfrag<5>(vr), pfm[1], a1);
            a0 = mfma16(colfrag<2>(vr), pfm[2], a0); a1 = mfma16(colfrag<6>(vr), pfm[2], a1);
            a0 = mfma16(colfrag<3>(vr), pfm[3], a0); a1 = mfma16(colfrag<7>(vr), pfm[3], a1);
            LAS unsigned char* pb = L + L_PART + dw * 2048 + c * 256 + (n & 12) * 16;
            *(LAS f32x4*)(pb + ((n + 2 * c) & 3) * 16) = a0;
            *(LAS f32x4*)(pb + 1024 + ((n + 2 * c + 1) & 3) * 16) = a1;
        }
        STAMP(4);
        BARRIER();
        if (dw < 4) {
            const int a = (lane >> 3) & 1, g = (lane >> 1) & 3;
            LAS unsigned char* pb = L + L_PART + a * 1024 + c * 256 + (g * 4 + ((dw + 2 * c + a) & 3)) * 16 + (lane & 1) * 8;
            f32x2 q0 = *(LAS f32x2*)(pb) + *(LAS f32x2*)(pb + 2048), q1 = *(LAS f32x2*)(pb + 2 * 2048) + *(LAS f32x2*)(pb + 3 * 2048);
            f32x2 q2 = *(LAS f32x2*)(pb + 4 * 2048) + *(LAS f32x2*)(pb + 5 * 2048), q3 = *(LAS f32x2*)(pb + 6 * 2048) + *(LAS f32x2*)(pb + 7 * 2048);
            q0 = (q0 + q1) + (q2 + q3);
            ST8((u32x2{pack2(q0[0], q0[1]), tag}), WS_O + (kvh * 32 + r) * 2048 + dw * 512 + lane * 8);
        }
        STAMP(5);
        float wt[8];
        {
            u32x4 g[1];
            gather<1, 0>(g, rs, WS_LSE + ((kvh * 4 + rho * 2 + (dw >> 2)) * 32 + c * 8 + (n & 3) * 2) * 8, 0, tag, ~0ull);
            const float l0 = __uint_as_float(g[0][0]), l1 = __uint_as_float(g[0][2]);
            float M = fmaxf(l0, l1);
            M = fmaxf(M, dpp<XOR1>(M)); M = fmaxf(M, dpp<XOR2>(M));
            const float x0 = __builtin_amdgcn_exp2f(l0 - M), x1 = __builtin_amdgcn_exp2f(l1 - M);
            float S = x0 + x1;
            S += dpp<XOR1>(S); S += dpp<XOR2>(S);
            const float inv = __builtin_amdgcn_rcpf(S);
            const float y0 = x0 * inv, y1 = x1 * inv;
            wt[0] = qbc<0>(y0); wt[1] = qbc<0>(y1); wt[2] = qbc<1>(y0); wt[3] = qbc<1>(y1);
            wt[4] = qbc<2>(y0); wt[5] = qbc<2>(y1); wt[6] = qbc<3>(y0); wt[7] = qbc<3>(y1);
        }
        u32x4 bf0, bf1;
        {
            LAS unsigned char* wb = L + L_WO + dw * 2048;
            *(LAS u32x4*)(wb + lane * 16) = rr[6];
            *(LAS u32x4*)(wb + 1024 + lane * 16) = rr[7];
            LGK0();
            LAS unsigned char* tb = wb + (8 * c + (n >> 2)) * 64 + (n & 3) * 8;
            bf0 = trfrag(tb + (c & 1) * 32, tb + 256 + (c & 1) * 32);
            bf1 = trfrag(tb + (~c & 1) * 32, tb + 256 + (~c & 1) * 32);
        }
        SB();
        STAMP(6);
        if (DA) __builtin_amdgcn_s_sleep(DA);
        {
            u32x4 a[4];
            gather<4, 0>(a, rs, WS_O + (kvh * 32 + (lane >> 3)) * 2048 + (rho * 8 + dw) * 128 + (lane & 7) * 16, 8 * 2048, tag, ~0ull);
            STAMP(7);
            LAS unsigned char* ab = L + L_AT + dw * 2048;
            LAS unsigned char* wa = ab + (lane >> 5) * 1024 + ((lane >> 3) & 3) * 64 + (lane & 1) * 8;
#pragma unroll
            for (int i = 0; i < 4; i++) *(LAS u32x2*)(wa + i * 256 + ((((lane >> 1) & 3) ^ i) * 16)) = u32x2{a[i][0], a[i][2]};
            LGK0();
            const u32x4 af0 = *(LAS u32x4*)(ab + n * 64 + (c ^ (n >> 2)) * 16);
            const u32x4 af1 = *(LAS u32x4*)(ab + 1024 + n * 64 + (c ^ (n >> 2)) * 16);
            const f32x4 z = {0.f, 0.f, 0.f, 0.f};
            const f32x4 o00 = mfma16(af0, bf0, z), o01 = mfma16(af0, bf1, z), o10 = mfma16(af1, bf0, z), o11 = mfma16(af1, bf1, z);
            f32x2 q0 = f32x2{o00[0], o01[0]} * wt[0], q1 = f32x2{o10[0], o11[0]} * wt[4];
#pragma unroll
            for (int i = 1; i < 4; i++) { q0 += f32x2{o00[i], o01[i]} * wt[i]; q1 += f32x2{o10[i], o11[i]} * wt[4 + i]; }
            *(LAS f32x2*)(L + L_R2 + dw * 512 + lane * 8) = q0 + q1;
        }
        BARRIER();
        if (dw == 0) {
            LAS unsigned char* pb = L + L_R2 + lane * 8;
            f32x2 q0 = *(LAS f32x2*)(pb) + *(LAS f32x2*)(pb + 512), q1 = *(LAS f32x2*)(pb + 1024) + *(LAS f32x2*)(pb + 1536);
            f32x2 q2 = *(LAS f32x2*)(pb + 2048) + *(LAS f32x2*)(pb + 2560), q3 = *(LAS f32x2*)(pb + 3072) + *(LAS f32x2*)(pb + 3584);
            q0 = (q0 + q1) + (q2 + q3);
            ST8((u32x2{pack2(q0[0], q0[1]), tag}), WS_P + c * 32768 + cg * 2048 + (kvh * 2 + rho) * 128 + n * 8);
        }
        STAMP(8);
        if (cons) {
            const int col = dw * 64 + (lane & 48) + (lane & 7) * 2 + ((lane >> 3) & 1);
            const float xv = bflo(*(LAS u16*)(L + L_X + bj * 1024 + col * 2));
            {
                u32x4 g[2];
                gather<2, 0>(g, rs, WS_TAB + (dw * 32 + (lane >> 2)) * 128 + (lane & 3) * 16, 16 * 128, tag, ~0ull);
                *(LAS u32x2*)(L + L_TAB + dw * 1024 + lane * 8) = u32x2{g[0][0], g[0][2]};
                *(LAS u32x2*)(L + L_TAB + dw * 1024 + 512 + lane * 8) = u32x2{g[1][0], g[1][2]};
            }
            if (DB) __builtin_amdgcn_s_sleep(DB);
            u32x4 a[4];
            gather<4, 0>(a, rs, WS_P + bj * 32768 + dw * 4096 + lane * 16, 1024, tag, ~0ull);
            STAMP(9);
            const float v0 = sw32(bflo(a[0][0]) + bflo(a[1][0]), bflo(a[2][0]) + bflo(a[3][0]));
            const float v1 = sw32(bfhi(a[0][0]) + bfhi(a[1][0]), bfhi(a[2][0]) + bfhi(a[3][0]));
            const float v2 = sw32(bflo(a[0][2]) + bflo(a[1][2]), bflo(a[2][2]) + bflo(a[3][2]));
            const float v3 = sw32(bfhi(a[0][2]) + bfhi(a[1][2]), bfhi(a[2][2]) + bfhi(a[3][2]));
            const float u0 = sw16(v0, v1), u1 = sw16(v2, v3);
            const float at = ((lane & 8) ? u1 : u0) + dpp<ROR8>((lane & 8) ? u0 : u1);
            const float h = rbf(xv + rbf(at));
            const float piv = __uint_as_float(__builtin_amdgcn_readfirstlane(__float_as_uint(h)));
            const float d = h - piv;
            const float s1 = wave_sum(d), s2 = wave_sum(d * d);
            if (lane == 0) *(LAS f32x2*)(L + L_S2 + dw * 8) = f32x2{fmaf(s1, 1.0f / 64, piv), fmaf(-s1 * (1.0f / 64), s1, s2)};
            BARRIER();
            const f32x2 ms = *(LAS f32x2*)(L + L_S2 + (lane & 7) * 8);
            float mean = ms[0];
            mean += dpp<XOR1>(mean); mean += dpp<XOR2>(mean); mean += dpp<HMIR>(mean);
            mean *= 0.125f;
            const float dv = ms[0] - mean;
            float m2 = fmaf(dv * 64.f, dv, ms[1]);
            m2 += dpp<XOR1>(m2); m2 += dpp<XOR2>(m2); m2 += dpp<HMIR>(m2);
            const float var = fmaxf(m2 * (1.0f / D), 0.f);
            const float m1 = rbf(mean);
            const float inv1 = __builtin_amdgcn_rcpf(rbf(__builtin_amdgcn_sqrtf(rbf(rbf(var) + EPS))));
            const float t1 = rbf(rbf(h - m1) * inv1);
            const float mb = rbf((mean - m1) * inv1);
            const float inv2 = __builtin_amdgcn_rcpf(rbf(__builtin_amdgcn_sqrtf(rbf(rbf(var * inv1 * inv1) + EPS))));
            const u32 tb = pack2(rbf(t1 - mb) * inv2, 0.f) & 0xFFFFu;
            const u32 ti = (tb & 0x7FFFu) - (TEXP0 << 7);
            float y = bflo(*(LAS u16*)(L + L_TAB + ((ti & 2047u) + (tb >> 15) * 2048u) * 2));
            bool miss = ti >= 2048u;
            u64 mm = __ballot(miss);
            while (__builtin_expect(mm != 0, 0)) {
                const u32 tv = __builtin_amdgcn_readlane(tb, __builtin_ctzll(mm));
                const float t = bflo(tv);
                f32x2 acc = {0.f, 0.f};
#pragma unroll 1
                for (int it = 0; it < 14; it++) {
                    LAS unsigned char* pp = L + L_PAR + it * 256 + lane * 4;
                    acc = unit2(t, un2(*(LAS u32*)pp), un2(*(LAS u32*)(pp + F * 2)), un2(*(LAS u32*)(pp + F * 4)), acc);
                }
                const float yy = rbf(rbf(wave_sum(acc[0] + acc[1])) + bflo(*(LAS u32*)(L + L_FLG + 32)));
                if (tb == tv) { y = yy; miss = false; }
                mm = __ballot(miss);
            }
            out[bj * D + col] = (u16)pack2(h + y, 0.f);
            if (wg == 0 && dw == 0 && lane == 0) __builtin_amdgcn_raw_buffer_store_b32(tag, rs, WS_EPOCH, 0, SC1);
            STAMP(10);
        }
        FIN();
    } else {
        if (dw < 4) {
            float v[8];
#pragma unroll
            for (int k = 0; k < 4; k++) { v[2 * k] = bflo(xr[k]); v[2 * k + 1] = bfhi(xr[k]); }
            const float mean = wave_sum(((v[0] + v[1]) + (v[2] + v[3])) + ((v[4] + v[5]) + (v[6] + v[7]))) * (1.0f / D);
            float ss = 0.f;
#pragma unroll
            for (int k = 0; k < 8; k++) { const float d = v[k] - mean; ss = fmaf(d, d, ss); }
            const float var = rbf(wave_sum(ss) * (1.0f / D));
            const float m = rbf(mean);
            const float inv = __builtin_amdgcn_rcpf(rbf(__builtin_amdgcn_sqrtf(rbf(var + EPS))));
            u32x4 o;
#pragma unroll
            for (int k = 0; k < 4; k++) o[k] = pack2(rbf(v[2 * k] - m) * inv, rbf(v[2 * k + 1] - m) * inv);
            *(LAS u32x4*)(L + L_XN + dw * 1024 + lane * 16) = o;
            *(LAS u32x4*)(L + L_X + dw * 1024 + lane * 16) = xr;
            LGK0();
            *(LAS u32*)(L + L_FLG + 16 + dw * 4) = tag;
        }
        STAMP(2);
        {
            LAS unsigned char* wb = L + L_WQ + dw * 2048;
            *(LAS u32x4*)(wb + lane * 16) = e0;
            *(LAS u32x4*)(wb + 1024 + lane * 16) = e1;
            LGK0();
            LAS unsigned char* tb = wb + (8 * c + (n >> 2)) * 32 + (n & 3) * 8;
            const u32x4 f0 = trfrag(tb, tb + 128), f1 = trfrag(tb + 1024, tb + 1152);
            STAMP(3);
#pragma unroll 1
            for (u32 spin = 0; spin < SPIN_CAP; spin++) {
                MEMB();
                const u32x4 f = *(LAS u32x4*)(L + L_FLG + 16);
                if (__builtin_amdgcn_readfirstlane((f[0] ^ tag) | (f[1] ^ tag) | (f[2] ^ tag) | (f[3] ^ tag)) == 0) break;
            }
            MEMB();
            LAS unsigned char* ab = L + L_XN + (n & 3) * 1024 + dw * 128 + c * 16;
            f32x4 qa = mfma16(*(LAS u32x4*)(ab), f0, f32x4{0.f, 0.f, 0.f, 0.f});
            qa = mfma16(*(LAS u32x4*)(ab + 64), f1, qa);
            const float qv = (c & 2) ? ((c & 1) ? qa[3] : qa[2]) : ((c & 1) ? qa[1] : qa[0]);
            *(LAS u32x2*)(L + L_QP + dw * 512 + lane * 8) = u32x2{__float_as_uint(qv), tag};
        }
        u32x4 pre = {0u, 0u, 0u, 0u};
        if (dw == 0) {
            u32x2 p[8];
#pragma unroll 1
            for (u32 spin = 0; spin < SPIN_CAP; spin++) {
                MEMB();
                u32 bad = 0;
#pragma unroll
                for (int i = 0; i < 8; i++) { p[i] = *(LAS u32x2*)(L + L_QP + i * 512 + lane * 8); bad |= p[i][1] ^ tag; }
                if (__all(bad == 0)) break;
            }
            const float qv = ((__uint_as_float(p[0][0]) + __uint_as_float(p[1][0])) + (__uint_as_float(p[2][0]) + __uint_as_float(p[3][0]))) +
                             ((__uint_as_float(p[4][0]) + __uint_as_float(p[5][0])) + (__uint_as_float(p[6][0]) + __uint_as_float(p[7][0])));
            ST8((u32x2{pack2(qv, dpp<XOR1>(qv)), tag}), (n & 1) ? OOB : WS_Q + (kvh * 32 + r) * 256 + c * 64 + n * 4);
            STAMP(4);
            __builtin_amdgcn_s_sleep(DQ);
            u32x4 g[2];
            gather<2, 0>(g, rs, WS_Q + (kvh * 32 + (lane >> 2)) * 256 + b * 64 + (lane & 3) * 16, 16 * 256, tag, ~0ull);
            *(LAS u32x2*)(L + L_Q + lane * 8) = u32x2{g[0][0], g[0][2]};
            *(LAS u32x2*)(L + L_Q + 512 + lane * 8) = u32x2{g[1][0], g[1][2]};
            LGK0();
            *(LAS u32*)(L + L_FLG) = tag;
            STAMP(5);
        } else {
            const int tw = dw - 1;
            *(LAS u32x2*)(L + L_PAR + tw * 512 + lane * 8) = pw1;
            *(LAS u32x2*)(L + L_PAR + F * 2 + tw * 512 + lane * 8) = pb1;
            *(LAS u32x2*)(L + L_PAR + F * 4 + tw * 512 + lane * 8) = pw2;
            *(LAS u32*)(L + L_FLG + 32) = b2v;
            const f32x2 w1a = un2(pw1[0]), w1b = un2(pw1[1]), b1a = un2(pb1[0]), b1b = un2(pb1[1]), w2a = un2(pw2[0]), w2b = un2(pw2[1]);
            const u32 t0 = ((u32)(wg & 128) << 24) + (((TEXP0 << 7) + (u32)(wg & 127) * 16) << 16);
#pragma unroll 1
            for (int it = 0; it < 4; it++) {
                float a[4];
#pragma unroll
                for (int k = 0; k < 4; k++) {
                    const float t = __uint_as_float(t0 + ((u32)(it * 4 + k) << 16));
                    const f32x2 q = unit2(t, w1b, b1b, w2b, unit2(t, w1a, b1a, w2a, f32x2{0.f, 0.f}));
                    a[k] = q[0] + q[1];
                }
                const float v = row_sum(sw16(sw32(a[0], a[2]), sw32(a[1], a[3])));
                if (n == 0) *(LAS u32x2*)(L + L_TP + tw * 128 + (it * 4 + c) * 8) = u32x2{__float_as_uint(v), tag};
            }
            STAMP(4);
#pragma unroll 1
            for (u32 spin = 0; spin < SPIN_CAP; spin++) {
                MEMB();
                if (__builtin_amdgcn_readfirstlane(*(LAS u32*)(L + L_FLG)) == tag) break;
                __builtin_amdgcn_s_sleep(2);
            }
            STAMP(5);
        }
        MEMB();
        {
            const u32x4 qf = *(LAS u32x4*)(L + L_Q + (n & 3) * 256 + c * 64 + (n >> 2) * 16);
            const f32x4 z = {0.f, 0.f, 0.f, 0.f};
            float sc[8];
#pragma unroll
            for (int j = 0; j < 8; j++) {
                const f32x4 t = mfma16(rr[j], qf, z);
                float v = (n & 8) ? ((n & 4) ? t[3] : t[2]) : ((n & 4) ? t[1] : t[0]);
                v += dpp<ROR4>(v); v += dpp<ROR8>(v);
                sc[j] = rbf(rbf(v) * 0.08838834764831845f) * LOG2E;
            }
            float mx = fmaxf(fmaxf(fmaxf(sc[0], sc[1]), fmaxf(sc[2], sc[3])), fmaxf(fmaxf(sc[4], sc[5]), fmaxf(sc[6], sc[7])));
            mx = x16_max(mx);
            float p[8];
#pragma unroll
            for (int j = 0; j < 8; j++) p[j] = __builtin_amdgcn_exp2f(sc[j] - mx);
            const float ls = x16_sum(((p[0] + p[1]) + (p[2] + p[3])) + ((p[4] + p[5]) + (p[6] + p[7])));
            if (n < 4) {
                *(LAS f32x4*)(L + L_PP + dw * 512 + (c * 4 + n) * 32) = f32x4{p[0], p[1], p[2], p[3]};
                *(LAS f32x4*)(L + L_PP + dw * 512 + (c * 4 + n) * 32 + 16) = f32x4{p[4], p[5], p[6], p[7]};
                *(LAS float*)(L + L_MX + n * 32 + dw * 4) = mx;
                *(LAS float*)(L + L_LS + n * 32 + dw * 4) = ls;
            }
        }
        STAMP(6);
        if (dw == 0) {
            u32x2 p0, p1;
#pragma unroll 1
            for (u32 spin = 0; spin < SPIN_CAP; spin++) {
                MEMB();
                p0 = *(LAS u32x2*)(L + L_TP + lane * 8);
                p1 = *(LAS u32x2*)(L + L_TP + 512 + lane * 8);
                if (__all(p0[1] == tag && (c == 3 || p1[1] == tag))) break;
            }
            const float y = x16_sum(__uint_as_float(p0[0]) + (c == 3 ? 0.f : __uint_as_float(p1[0])));
            const float yy = rbf(y) + bflo(b2v);
            ST8((u32x2{pack2(yy, dpp<XOR1>(yy)), tag}), (lane & 49) ? OOB : WS_TAB + wg * 128 + lane * 4);
        }
        asm volatile("" :: "v"(pre));
        LGK0();
        STAMP(7);
        FIN();
        asm volatile(".Lc_end:");
    }
}

extern "C" void kernel_launch(void* const* d_in, const int* in_sizes, int n_in,
                              void* d_out, int out_size, void* d_ws, size_t ws_size,
                              hipStream_t stream) {
    (void)in_sizes; (void)n_in; (void)out_size; (void)ws_size;
    (void)hipFuncSetAttribute((const void*)fused_kernel, hipFuncAttributeMaxDynamicSharedMemorySize, LDS_BYTES);
    fused_kernel<<<256, 1024, LDS_BYTES, stream>>>(
        (const u16*)d_in[1], (const u16*)d_in[2], (unsigned char*)d_ws, (const u16*)d_in[3],
        (const u16*)d_in[0], (const u16*)d_in[4],
        (const u16*)d_in[6], (const u16*)d_in[7], (const u16*)d_in[8], (const u16*)d_in[9],
        (u16*)d_out);
}
```

```cpp
#define PROBE 19
#include <hip/hip_runtime.h>
#include <stdint.h>

typedef unsigned short u16;
typedef unsigned int u32;
typedef unsigned long long u64;
typedef __attribute__((ext_vector_type(4))) u32 u32x4;
typedef __attribute__((ext_vector_type(2))) u32 u32x2;
typedef __attribute__((ext_vector_type(4))) float f32x4;
typedef __attribute__((ext_vector_type(2))) float f32x2;
typedef __attribute__((ext_vector_type(8))) short bf16x8;
typedef __attribute__((ext_vector_type(4))) short s16x4;
typedef __attribute__((ext_vector_type(2))) __bf16 bf16x2;
#define LAS __attribute__((address_space(3)))
#define GAS __attribute__((address_space(1)))

#define WIN 7
#define DQ 8
#define DL 8
#define DA 3
#define DB 8
#define CODE_KB 7

constexpr int D = 512, SK = 8192, DH = 128, W = 2048, F = 1792;
constexpr float EPS = 1e-5f;
constexpr float LOG2E = 1.4426950408889634f;
constexpr u32 SPIN_CAP = 1u << 18;
constexpr u32 TEXP0 = 114;

constexpr int WS_EPOCH = 0;
constexpr int WS_Q   = 4096;
constexpr int WS_LSE = WS_Q + 65536;
constexpr int WS_TAB = WS_LSE + 8192;
constexpr int WS_P   = WS_TAB + 32768;
constexpr int WS_O   = WS_P + 131072;
constexpr int WS_END = WS_O + 524288;
constexpr size_t WS_STAMP = 64u << 20;
constexpr int OOB = 0x7ffffff0;

constexpr int L_WQ   = 0;
constexpr int L_XN   = 16384;
constexpr int L_X    = L_XN + 4096;
constexpr int L_Q    = L_X + 4096;
constexpr int L_FLG  = L_Q + 1024;
constexpr int L_MX   = L_FLG + 64;
constexpr int L_LS   = L_MX + 128;
constexpr int L_TP   = L_LS + 128;
constexpr int L_S2   = L_TP + 1024;
constexpr int L_QP   = L_S2 + 64;
constexpr int L_PP   = L_QP + 4096;
constexpr int L_R2   = L_PP + 4096;
constexpr int L_PART = L_R2 + 4096;
constexpr int L_WO   = L_PART + 16384;
constexpr int L_AT   = L_WO + 16384;
constexpr int L_TAB  = L_AT + 16384;
constexpr int L_PAR  = L_TAB + 8192;
constexpr int L_END  = L_PAR + 3 * F * 2;
constexpr int LDS_BYTES = 108 * 1024;
static_assert(L_END <= LDS_BYTES, "lds");

__device__ __forceinline__ float bflo(u32 v) { return __uint_as_float(v << 16); }
__device__ __forceinline__ float bfhi(u32 v) { return __uint_as_float(v & 0xFFFF0000u); }
__device__ __forceinline__ u32 pack2(float lo, float hi) {
    bf16x2 v = {(__bf16)lo, (__bf16)hi};
    return __builtin_bit_cast(u32, v);
}
__device__ __forceinline__ float rbf(float x) { return (float)(__bf16)x; }
template <int CTRL> __device__ __forceinline__ float dpp(float x) {
    return __builtin_bit_cast(float, __builtin_amdgcn_mov_dpp(__builtin_bit_cast(int, x), CTRL, 0xf, 0xf, true));
}
__device__ __forceinline__ float sw32(float a, float b) {
    auto t = __builtin_amdgcn_permlane32_swap(__float_as_uint(a), __float_as_uint(b), false, false);
    return __uint_as_float(t[0]) + __uint_as_float(t[1]);
}
__device__ __forceinline__ float sw16(float a, float b) {
    auto t = __builtin_amdgcn_permlane16_swap(__float_as_uint(a), __float_as_uint(b), false, false);
    return __uint_as_float(t[0]) + __uint_as_float(t[1]);
}
__device__ __forceinline__ float x16_sum(float x) { x = sw16(x, x); return sw32(x, x); }
__device__ __forceinline__ float x16_max(float x) {
    auto s = __builtin_amdgcn_permlane16_swap(__float_as_uint(x), __float_as_uint(x), false, false);
    x = fmaxf(__uint_as_float(s[0]), __uint_as_float(s[1]));
    auto t = __builtin_amdgcn_permlane32_swap(__float_as_uint(x), __float_as_uint(x), false, false);
    return fmaxf(__uint_as_float(t[0]), __uint_as_float(t[1]));
}
constexpr int XOR1 = 0xB1, XOR2 = 0x4E, ROR4 = 0x124, ROR8 = 0x128, HMIR = 0x141;
__device__ __forceinline__ float row_sum(float x) {
    x += dpp<XOR1>(x); x += dpp<XOR2>(x); x += dpp<ROR4>(x); x += dpp<ROR8>(x);
    return x;
}
__device__ __forceinline__ float wave_sum(float x) { return x16_sum(row_sum(x)); }
__device__ __forceinline__ f32x4 mfma16(u32x4 a, u32x4 b, f32x4 c) {
    return __builtin_amdgcn_mfma_f32_16x16x32_bf16(__builtin_bit_cast(bf16x8, a), __builtin_bit_cast(bf16x8, b), c, 0, 0, 0);
}
__device__ __forceinline__ u32x4 ldnt(const u16* p) { return __builtin_nontemporal_load((const u32x4*)p); }
__device__ __forceinline__ u32x4 trfrag(LAS unsigned char* lo, LAS unsigned char* hi) {
    const u32x2 l = __builtin_bit_cast(u32x2, __builtin_amdgcn_ds_read_tr16_b64_v4i16((LAS s16x4*)lo));
    const u32x2 h = __builtin_bit_cast(u32x2, __builtin_amdgcn_ds_read_tr16_b64_v4i16((LAS s16x4*)hi));
    return u32x4{l[0], l[1], h[0], h[1]};
}
#define SB() __builtin_amdgcn_sched_barrier(0)
#define MEMB() asm volatile("" ::: "memory")
#define BARRIER() do { asm volatile("s_waitcnt lgkmcnt(0)\n\ts_barrier" ::: "memory"); } while (0)
#define LGK0() asm volatile("s_waitcnt lgkmcnt(0)" ::: "memory")
constexpr int SC1 = 16, NT = 2;
#define LD16(off) __builtin_amdgcn_raw_buffer_load_b128(rs, off, 0, SC1)
#define ST8(v, off) __builtin_amdgcn_raw_buffer_store_b64(v, rs, off, 0, SC1)

#define G_LD(i, so) "s_mov_b64 exec, %[m" #i "]\n\tbuffer_load_dwordx4 %[a" #i "], %[vo], %[rs], " so " offen sc1\n\t"
#define G_END "s_mov_b64 exec, -1\n\ts_waitcnt vmcnt(0)"
template <int N, int NAP>
__device__ __forceinline__ void gather(u32x4 (&a)[N], __amdgpu_buffer_rsrc_t rs, int off, int stride, u32 tag, u64 act) {
    u64 m[N];
#pragma unroll
    for (int i = 0; i < N; i++) { m[i] = act; asm volatile("" : "=v"(a[i])); }
#pragma unroll 1
    for (u32 spin = 0; spin < SPIN_CAP; spin++) {
        if constexpr (N == 4)
            asm volatile(G_LD(0, "0") G_LD(1, "%[s1]") G_LD(2, "%[s2]") G_LD(3, "%[s3]") G_END
                : [a0] "+v"(a[0]), [a1] "+v"(a[1]), [a2] "+v"(a[2]), [a3] "+v"(a[3])
                : [m0] "s"(m[0]), [m1] "s"(m[1]), [m2] "s"(m[2]), [m3] "s"(m[3]), [vo] "v"(off), [rs] "s"(rs),
                  [s1] "s"(stride), [s2] "s"(2 * stride), [s3] "s"(3 * stride) : "memory");
        else if constexpr (N == 2)
            asm volatile(G_LD(0, "0") G_LD(1, "%[s1]") G_END
                : [a0] "+v"(a[0]), [a1] "+v"(a[1])
                : [m0] "s"(m[0]), [m1] "s"(m[1]), [vo] "v"(off), [rs] "s"(rs), [s1] "s"(stride) : "memory");
        else
            asm volatile(G_LD(0, "0") G_END : [a0] "+v"(a[0]) : [m0] "s"(m[0]), [vo] "v"(off), [rs] "s"(rs) : "memory");
        u64 any = 0;
#pragma unroll
        for (int i = 0; i < N; i++) { m[i] = __ballot(a[i][1] != tag || a[i][3] != tag) & act; any |= m[i]; }
        if (!any) break;
        if (NAP) __builtin_amdgcn_s_sleep(NAP);
    }
}

template <int DD> __device__ __forceinline__ u32x4 colfrag(const u32x4 (&r)[8]) {
    constexpr u32 sel = (DD & 1) ? 0x07060302u : 0x05040100u;
    u32x4 f;
#pragma unroll
    for (int w = 0; w < 4; w++) f[w] = __builtin_amdgcn_perm(r[2 * w + 1][DD >> 1], r[2 * w][DD >> 1], sel);
    return f;
}

constexpr float GK0 = -2.0f * 0.7978845608028654f * LOG2E, GK1 = GK0 * 0.044715f;
__device__ __forceinline__ f32x2 unit2(float t, f32x2 w1, f32x2 b1, f32x2 w2, f32x2 acc) {
    const f32x2 u = __builtin_elementwise_fma(f32x2{t, t}, w1, b1);
    const f32x2 z = u * __builtin_elementwise_fma(u * u, f32x2{GK1, GK1}, f32x2{GK0, GK0});
    const f32x2 e = f32x2{__builtin_amdgcn_exp2f(z[0]), __builtin_amdgcn_exp2f(z[1])} + f32x2{1.f, 1.f};
    const f32x2 r = {__builtin_amdgcn_rcpf(e[0]), __builtin_amdgcn_rcpf(e[1])};
    return __builtin_elementwise_fma(u * w2, r, acc);
}
__device__ __forceinline__ f32x2 un2(u32 v) { return f32x2{bflo(v), bfhi(v)}; }

#define STAMP(k) do { if (k == 0 || k == PROBE % 16) asm volatile("s_memrealtime %0\n\ts_waitcnt lgkmcnt(0)" : "=s"(st[k]) :: "memory"); } while (0)
#define FIN() do { if (PROBE >= 16 && wave == 0) { *(LAS u32*)(L + L_FLG + 48) = (u32)(st[PROBE % 16] - st[0]); LGK0(); } \
    if (wg == 0 && wave == 8) { u32 dt = PROBE >= 16 ? __builtin_amdgcn_readfirstlane(*(LAS u32*)(L + L_FLG + 48)) : (u32)(st[PROBE % 16] - st[0]); \
        u64 t0, t1; asm volatile("s_memrealtime %0\n\ts_waitcnt lgkmcnt(0)" : "=s"(t0) :: "memory"); t1 = t0; \
        while ((u32)(t1 - t0) < 4 * dt && dt < 100000u) asm volatile("s_memrealtime %0\n\ts_waitcnt lgkmcnt(0)" : "=s"(t1) :: "memory"); } } while (0)

#define RSRC(p, nbytes) __builtin_amdgcn_make_buffer_rsrc((void*)(p), 0, nbytes, 0x00020000)
#define LDNT(r, v, s) __builtin_amdgcn_raw_buffer_load_b128(r, v, s, NT)
template <int K> __device__ __forceinline__ float qbc(float x) { return dpp<K * 0x55>(x); }

extern "C" __global__ void __launch_bounds__(1024, 4)
fused_kernel(const u16* __restrict__ kc, const u16* __restrict__ vc, unsigned char* ws, const u16* __restrict__ wq,
             const u16* __restrict__ x, const u16* __restrict__ wo,
             const u16* __restrict__ w1, const u16* __restrict__ b1, const u16* __restrict__ w2,
             const u16* __restrict__ b2, u16* out) {
    extern __shared__ __attribute__((aligned(16))) unsigned char smem[];
    LAS unsigned char* L = (LAS unsigned char*)smem;
    const int wg = blockIdx.x, kvh = wg & 7, r = wg >> 3, b = r >> 3, s = r & 7;
    const int lane = threadIdx.x & 63;
    const int wave = __builtin_amdgcn_readfirstlane(threadIdx.x >> 6);
    const int c = lane >> 4, n = lane & 15, dw = wave & 7;
    u64 st[12] = {};
    STAMP(0);
    asm volatile("" :: "s"(kc), "s"(vc), "s"(ws), "s"(wq), "s"(x), "s"(wo), "s"(w1), "s"(b1), "s"(w2), "s"(b2), "s"(out));
    u32 ep = *(const __attribute__((address_space(4))) u32*)(ws + WS_EPOCH);
    const __amdgpu_buffer_rsrc_t rs = RSRC(ws, WS_END);
    const bool cons = (wg & 0xF9) == 0;
    const int bj = wg >> 1;
    const int rho = r & 1, cg = r >> 1;
    const bool kw = wave < 8;

    u32x4 xr, e0, e1, rr[8];
    u32x2 pw1, pb1, pw2;
    u32 b2v;
    if (kw) {
        __builtin_amdgcn_s_setprio(1);
        xr = *(const u32x4*)(x + (dw & 3) * D + lane * 8);
    }
    {
        const size_t base = ((size_t)(b * 8 + kvh) * SK + (SK - W) + s * 256 + dw * 32) * DH;
        const __amdgpu_buffer_rsrc_t rkv = RSRC((kw ? kc : vc) + base, 8192);
        const int kvo = kw ? (n >> 2) * 256 + c * 64 + (n & 3) * 16 : lane * 16;
        const __amdgpu_buffer_rsrc_t rw = RSRC(kw ? wq + (size_t)dw * 64 * 4096 + kvh * 512 + r * 16
                                                  : wo + (size_t)(kvh * 512 + rho * 256 + dw * 32) * D + cg * 32, kw ? 63 * 8192 + 32 : 31 * 1024 + 64);
        const int sg = lane >> 2, cp = sg >> 3;
        const int wof = kw ? (lane >> 1) * 8192 + (lane & 1) * 16 : (8 * (sg & 3) + 2 * cp + ((sg >> 2) & 1)) * 1024 + ((lane & 3) ^ (2 * cp)) * 16;
        const int wst = kw ? 32 * 8192 : 4096;
        const __amdgpu_buffer_rsrc_t ra = kw ? rw : rkv, rb = kw ? rkv : rw;
        const int ao = kw ? wof : kvo, bo = kw ? kvo : wof, s0 = kw ? 0 : 2048;
        e0 = LDNT(ra, ao, 0); SB();
        e1 = LDNT(ra, ao, kw ? wst : 1024); SB();
        if (kw) {
            const int po = (dw ? dw - 1 : 0) * 256 + lane * 4;
            pw1 = *(const u32x2*)(w1 + po);
            pb1 = *(const u32x2*)(b1 + po);
            pw2 = *(const u32x2*)(w2 + po);
            b2v = b2[0];
            SB();
            __builtin_amdgcn_s_setprio(0);
        }
#pragma unroll
        for (int j = 0; j < 6; j++) { rr[j] = LDNT(rkv, kvo + (j & 3) * 1024, s0 + (j >> 2) * 4096); SB(); }
        rr[6] = LDNT(rb, bo, kw ? 6144 : 0); SB();
        rr[7] = LDNT(rb, bo, kw ? 7168 : wst); SB();
    }
    int wv2 = wave;
    asm volatile("" : "+s"(ep), "+s"(wv2));
    const u32 tag = ep + 0x9E3779B9u;
    STAMP(1);

    if (__builtin_expect(wv2 >= 8, 1)) {
        BARRIER();
        __builtin_amdgcn_s_setprio(2);
        STAMP(2);
        u32x4 pfm[4];
        {
            const int h = n & 3;
            const f32x4 m0 = *(LAS f32x4*)(L + L_MX + h * 32), m1 = *(LAS f32x4*)(L + L_MX + h * 32 + 16);
            const f32x4 l0 = *(LAS f32x4*)(L + L_LS + h * 32), l1 = *(LAS f32x4*)(L + L_LS + h * 32 + 16);
            const float mo = *(LAS float*)(L + L_MX + h * 32 + dw * 4);
            const f32x4 p0 = *(LAS f32x4*)(L + L_PP + dw * 512 + (c * 4 + h) * 32), p1 = *(LAS f32x4*)(L + L_PP + dw * 512 + (c * 4 + h) * 32 + 16);
            const float M = fmaxf(fmaxf(fmaxf(m0[0], m0[1]), fmaxf(m0[2], m0[3])), fmaxf(fmaxf(m1[0], m1[1]), fmaxf(m1[2], m1[3])));
            float ls = 0.f;
#pragma unroll
            for (int i = 0; i < 4; i++) {
                ls = fmaf(l0[i], __builtin_amdgcn_exp2f(m0[i] - M), ls);
                ls = fmaf(l1[i], __builtin_amdgcn_exp2f(m1[i] - M), ls);
            }
            const float sc = __builtin_amdgcn_exp2f(mo - M) * __builtin_amdgcn_rcpf(ls);
            const u32x4 pf = {pack2(p0[0] * sc, p0[1] * sc), pack2(p0[2] * sc, p0[3] * sc), pack2(p1[0] * sc, p1[1] * sc), pack2(p1[2] * sc, p1[3] * sc)};
            const u32x4 zz = {0u, 0u, 0u, 0u};
#pragma unroll
            for (int g = 0; g < 4; g++) pfm[g] = (n >> 2) == g ? pf : zz;
            const float lse = M + __builtin_amdgcn_logf(ls);
            ST8((u32x2{__float_as_uint(lse), tag}), (dw == 7 && lane < 4) ? WS_LSE + ((kvh * 4 + lane) * 32 + r) * 8 : OOB);
        }
        SB();
        STAMP(3);
        {
            const u32x4 vr[8] = {e0, e1, rr[0], rr[1], rr[2], rr[3], rr[4], rr[5]};
            const f32x4 z = {0.f, 0.f, 0.f, 0.f};
            f32x4 a0 = mfma16(colfrag<0>(vr), pfm[0], z), a1 = mfma16(colfrag<4>(vr), pfm[0], z);
            a0 = mfma16(colfrag<1>(vr), pfm[1], a0); a1 = mfma16(colfrag<5>(vr), pfm[1], a1);
            a0 = mfma16(colfrag<2>(vr), pfm[2], a0); a1 = mfma16(colfrag<6>(vr), pfm[2], a1);
            a0 = mfma16(colfrag<3>(vr), pfm[3], a0); a1 = mfma16(colfrag<7>(vr), pfm[3], a1);
            LAS unsigned char* pb = L + L_PART + dw * 2048 + c * 256 + (n & 12) * 16;
            *(LAS f32x4*)(pb + ((n + 2 * c) & 3) * 16) = a0;
            *(LAS f32x4*)(pb + 1024 + ((n + 2 * c + 1) & 3) * 16) = a1;
        }
        STAMP(4);
        BARRIER();
        if (dw < 4) {
            const int a = (lane >> 3) & 1, g = (lane >> 1) & 3;
            LAS unsigned char* pb = L + L_PART + a * 1024 + c * 256 + (g * 4 + ((dw + 2 * c + a) & 3)) * 16 + (lane & 1) * 8;
            f32x2 q0 = *(LAS f32x2*)(pb) + *(LAS f32x2*)(pb + 2048), q1 = *(LAS f32x2*)(pb + 2 * 2048) + *(LAS f32x2*)(pb + 3 * 2048);
            f32x2 q2 = *(LAS f32x2*)(pb + 4 * 2048) + *(LAS f32x2*)(pb + 5 * 2048), q3 = *(LAS f32x2*)(pb + 6 * 2048) + *(LAS f32x2*)(pb + 7 * 2048);
            q0 = (q0 + q1) + (q2 + q3);
            ST8((u32x2{pack2(q0[0], q0[1]), tag}), WS_O + (kvh * 32 + r) * 2048 + dw * 512 + lane * 8);
        }
        STAMP(5);
        float wt[8];
        {
            u32x4 g[1];
            gather<1, 0>(g, rs, WS_LSE + ((kvh * 4 + rho * 2 + (dw >> 2)) * 32 + c * 8 + (n & 3) * 2) * 8, 0, tag, ~0ull);
            const float l0 = __uint_as_float(g[0][0]), l1 = __uint_as_float(g[0][2]);
            float M = fmaxf(l0, l1);
            M = fmaxf(M, dpp<XOR1>(M)); M = fmaxf(M, dpp<XOR2>(M));
            const float x0 = __builtin_amdgcn_exp2f(l0 - M), x1 = __builtin_amdgcn_exp2f(l1 - M);
            float S = x0 + x1;
            S += dpp<XOR1>(S); S += dpp<XOR2>(S);
            const float inv = __builtin_amdgcn_rcpf(S);
            const float y0 = x0 * inv, y1 = x1 * inv;
            wt[0] = qbc<0>(y0); wt[1] = qbc<0>(y1); wt[2] = qbc<1>(y0); wt[3] = qbc<1>(y1);
            wt[4] = qbc<2>(y0); wt[5] = qbc<2>(y1); wt[6] = qbc<3>(y0); wt[7] = qbc<3>(y1);
        }
        u32x4 bf0, bf1;
        {
            LAS unsigned char* wb = L + L_WO + dw * 2048;
            *(LAS u32x4*)(wb + lane * 16) = rr[6];
            *(LAS u32x4*)(wb + 1024 + lane * 16) = rr[7];
            LGK0();
            LAS unsigned char* tb = wb + (8 * c + (n >> 2)) * 64 + (n & 3) * 8;
            bf0 = trfrag(tb + (c & 1) * 32, tb + 256 + (c & 1) * 32);
            bf1 = trfrag(tb + (~c & 1) * 32, tb + 256 + (~c & 1) * 32);
        }
        SB();
        STAMP(6);
        if (DA) __builtin_amdgcn_s_sleep(DA);
        {
            u32x4 a[4];
            gather<4, 0>(a, rs, WS_O + (kvh * 32 + (lane >> 3)) * 2048 + (rho * 8 + dw) * 128 + (lane & 7) * 16, 8 * 2048, tag, ~0ull);
            STAMP(7);
            LAS unsigned char* ab = L + L_AT + dw * 2048;
            LAS unsigned char* wa = ab + (lane >> 5) * 1024 + ((lane >> 3) & 3) * 64 + (lane & 1) * 8;
#pragma unroll
            for (int i = 0; i < 4; i++) *(LAS u32x2*)(wa + i * 256 + ((((lane >> 1) & 3) ^ i) * 16)) = u32x2{a[i][0], a[i][2]};
            LGK0();
            const u32x4 af0 = *(LAS u32x4*)(ab + n * 64 + (c ^ (n >> 2)) * 16);
            const u32x4 af1 = *(LAS u32x4*)(ab + 1024 + n * 64 + (c ^ (n >> 2)) * 16);
            const f32x4 z = {0.f, 0.f, 0.f, 0.f};
            const f32x4 o00 = mfma16(af0, bf0, z), o01 = mfma16(af0, bf1, z), o10 = mfma16(af1, bf0, z), o11 = mfma16(af1, bf1, z);
            f32x2 q0 = f32x2{o00[0], o01[0]} * wt[0], q1 = f32x2{o10[0], o11[0]} * wt[4];
#pragma unroll
            for (int i = 1; i < 4; i++) { q0 += f32x2{o00[i], o01[i]} * wt[i]; q1 += f32x2{o10[i], o11[i]} * wt[4 + i]; }
            *(LAS f32x2*)(L + L_R2 + dw * 512 + lane * 8) = q0 + q1;
        }
        BARRIER();
        if (dw == 0) {
            LAS unsigned char* pb = L + L_R2 + lane * 8;
            f32x2 q0 = *(LAS f32x2*)(pb) + *(LAS f32x2*)(pb + 512), q1 = *(LAS f32x2*)(pb + 1024) + *(LAS f32x2*)(pb + 1536);
            f32x2 q2 = *(LAS f32x2*)(pb + 2048) + *(LAS f32x2*)(pb + 2560), q3 = *(LAS f32x2*)(pb + 3072) + *(LAS f32x2*)(pb + 3584);
            q0 = (q0 + q1) + (q2 + q3);
            ST8((u32x2{pack2(q0[0], q0[1]), tag}), WS_P + c * 32768 + cg * 2048 + (kvh * 2 + rho) * 128 + n * 8);
        }
        STAMP(8);
        if (cons) {
            const int col = dw * 64 + (lane & 48) + (lane & 7) * 2 + ((lane >> 3) & 1);
            const float xv = bflo(*(LAS u16*)(L + L_X + bj * 1024 + col * 2));
            {
                u32x4 g[2];
                gather<2, 0>(g, rs, WS_TAB + (dw * 32 + (lane >> 2)) * 128 + (lane & 3) * 16, 16 * 128, tag, ~0ull);
                *(LAS u32x2*)(L + L_TAB + dw * 1024 + lane * 8) = u32x2{g[0][0], g[0][2]};
                *(LAS u32x2*)(L + L_TAB + dw * 1024 + 512 + lane * 8) = u32x2{g[1][0], g[1][2]};
            }
            if (DB) __builtin_amdgcn_s_sleep(DB);
            u32x4 a[4];
            gather<4, 0>(a, rs, WS_P + bj * 32768 + dw * 4096 + lane * 16, 1024, tag, ~0ull);
            STAMP(9);
            const float v0 = sw32(bflo(a[0][0]) + bflo(a[1][0]), bflo(a[2][0]) + bflo(a[3][0]));
            const float v1 = sw32(bfhi(a[0][0]) + bfhi(a[1][0]), bfhi(a[2][0]) + bfhi(a[3][0]));
            const float v2 = sw32(bflo(a[0][2]) + bflo(a[1][2]), bflo(a[2][2]) + bflo(a[3][2]));
            const float v3 = sw32(bfhi(a[0][2]) + bfhi(a[1][2]), bfhi(a[2][2]) + bfhi(a[3][2]));
            const float u0 = sw16(v0, v1), u1 = sw16(v2, v3);
            const float at = ((lane & 8) ? u1 : u0) + dpp<ROR8>((lane & 8) ? u0 : u1);
            const float h = rbf(xv + rbf(at));
            const float piv = __uint_as_float(__builtin_amdgcn_readfirstlane(__float_as_uint(h)));
            const float d = h - piv;
            const float s1 = wave_sum(d), s2 = wave_sum(d * d);
            if (lane == 0) *(LAS f32x2*)(L + L_S2 + dw * 8) = f32x2{fmaf(s1, 1.0f / 64, piv), fmaf(-s1 * (1.0f / 64), s1, s2)};
            BARRIER();
            const f32x2 ms = *(LAS f32x2*)(L + L_S2 + (lane & 7) * 8);
            float mean = ms[0];
            mean += dpp<XOR1>(mean); mean += dpp<XOR2>(mean); mean += dpp<HMIR>(mean);
            mean *= 0.125f;
            const float dv = ms[0] - mean;
            float m2 = fmaf(dv * 64.f, dv, ms[1]);
            m2 += dpp<XOR1>(m2); m2 += dpp<XOR2>(m2); m2 += dpp<HMIR>(m2);
            const float var = fmaxf(m2 * (1.0f / D), 0.f);
            const float m1 = rbf(mean);
            const float inv1 = __builtin_amdgcn_rcpf(rbf(__builtin_amdgcn_sqrtf(rbf(rbf(var) + EPS))));
            const float t1 = rbf(rbf(h - m1) * inv1);
            const float mb = rbf((mean - m1) * inv1);
            const float inv2 = __builtin_amdgcn_rcpf(rbf(__builtin_amdgcn_sqrtf(rbf(rbf(var * inv1 * inv1) + EPS))));
            const u32 tb = pack2(rbf(t1 - mb) * inv2, 0.f) & 0xFFFFu;
            const u32 ti = (tb & 0x7FFFu) - (TEXP0 << 7);
            float y = bflo(*(LAS u16*)(L + L_TAB + ((ti & 2047u) + (tb >> 15) * 2048u) * 2));
            bool miss = ti >= 2048u;
            u64 mm = __ballot(miss);
            while (__builtin_expect(mm != 0, 0)) {
                const u32 tv = __builtin_amdgcn_readlane(tb, __builtin_ctzll(mm));
                const float t = bflo(tv);
                f32x2 acc = {0.f, 0.f};
#pragma unroll 1
                for (int it = 0; it < 14; it++) {
                    LAS unsigned char* pp = L + L_PAR + it * 256 + lane * 4;
                    acc = unit2(t, un2(*(LAS u32*)pp), un2(*(LAS u32*)(pp + F * 2)), un2(*(LAS u32*)(pp + F * 4)), acc);
                }
                const float yy = rbf(rbf(wave_sum(acc[0] + acc[1])) + bflo(*(LAS u32*)(L + L_FLG + 32)));
                if (tb == tv) { y = yy; miss = false; }
                mm = __ballot(miss);
            }
            out[bj * D + col] = (u16)pack2(h + y, 0.f);
            if (wg == 0 && dw == 0 && lane == 0) __builtin_amdgcn_raw_buffer_store_b32(tag, rs, WS_EPOCH, 0, SC1);
            STAMP(10);
        }
        FIN();
    } else {
        if (dw < 4) {
            float v[8];
#pragma unroll
            for (int k = 0; k < 4; k++) { v[2 * k] = bflo(xr[k]); v[2 * k + 1] = bfhi(xr[k]); }
            const float mean = wave_sum(((v[0] + v[1]) + (v[2] + v[3])) + ((v[4] + v[5]) + (v[6] + v[7]))) * (1.0f / D);
            float ss = 0.f;
#pragma unroll
            for (int k = 0; k < 8; k++) { const float d = v[k] - mean; ss = fmaf(d, d, ss); }
            const float var = rbf(wave_sum(ss) * (1.0f / D));
            const float m = rbf(mean);
            const float inv = __builtin_amdgcn_rcpf(rbf(__builtin_amdgcn_sqrtf(rbf(var + EPS))));
            u32x4 o;
#pragma unroll
            for (int k = 0; k < 4; k++) o[k] = pack2(rbf(v[2 * k] - m) * inv, rbf(v[2 * k + 1] - m) * inv);
            *(LAS u32x4*)(L + L_XN + dw * 1024 + lane * 16) = o;
            *(LAS u32x4*)(L + L_X + dw * 1024 + lane * 16) = xr;
            LGK0();
            *(LAS u32*)(L + L_FLG + 16 + dw * 4) = tag;
        }
        STAMP(2);
        {
            LAS unsigned char* wb = L + L_WQ + dw * 2048;
            *(LAS u32x4*)(wb + lane * 16) = e0;
            *(LAS u32x4*)(wb + 1024 + lane * 16) = e1;
            LGK0();
            LAS unsigned char* tb = wb + (8 * c + (n >> 2)) * 32 + (n & 3) * 8;
            const u32x4 f0 = trfrag(tb, tb + 128), f1 = trfrag(tb + 1024, tb + 1152);
            STAMP(3);
#pragma unroll 1
            for (u32 spin = 0; spin < SPIN_CAP; spin++) {
                MEMB();
                const u32x4 f = *(LAS u32x4*)(L + L_FLG + 16);
                if (__builtin_amdgcn_readfirstlane((f[0] ^ tag) | (f[1] ^ tag) | (f[2] ^ tag) | (f[3] ^ tag)) == 0) break;
            }
            MEMB();
            LAS unsigned char* ab = L + L_XN + (n & 3) * 1024 + dw * 128 + c * 16;
            f32x4 qa = mfma16(*(LAS u32x4*)(ab), f0, f32x4{0.f, 0.f, 0.f, 0.f});
            qa = mfma16(*(LAS u32x4*)(ab + 64), f1, qa);
            const float qv = (c & 2) ? ((c & 1) ? qa[3] : qa[2]) : ((c & 1) ? qa[1] : qa[0]);
            *(LAS u32x2*)(L + L_QP + dw * 512 + lane * 8) = u32x2{__float_as_uint(qv), tag};
        }
        u32x4 pre = {0u, 0u, 0u, 0u};
        if (dw == 0) {
            u32x2 p[8];
#pragma unroll 1
            for (u32 spin = 0; spin < SPIN_CAP; spin++) {
                MEMB();
                u32 bad = 0;
#pragma unroll
                for (int i = 0; i < 8; i++) { p[i] = *(LAS u32x2*)(L + L_QP + i * 512 + lane * 8); bad |= p[i][1] ^ tag; }
                if (__all(bad == 0)) break;
            }
            const float qv = ((__uint_as_float(p[0][0]) + __uint_as_float(p[1][0])) + (__uint_as_float(p[2][0]) + __uint_as_float(p[3][0]))) +
                             ((__uint_as_float(p[4][0]) + __uint_as_float(p[5][0])) + (__uint_as_float(p[6][0]) + __uint_as_float(p[7][0])));
            ST8((u32x2{pack2(qv, dpp<XOR1>(qv)), tag}), (n & 1) ? OOB : WS_Q + (kvh * 32 + r) * 256 + c * 64 + n * 4);
            STAMP(4);
            __builtin_amdgcn_s_sleep(DQ);
            u32x4 g[2];
            gather<2, 0>(g, rs, WS_Q + (kvh * 32 + (lane >> 2)) * 256 + b * 64 + (lane & 3) * 16, 16 * 256, tag, ~0ull);
            *(LAS u32x2*)(L + L_Q + lane * 8) = u32x2{g[0][0], g[0][2]};
            *(LAS u32x2*)(L + L_Q + 512 + lane * 8) = u32x2{g[1][0], g[1][2]};
            LGK0();
            *(LAS u32*)(L + L_FLG) = tag;
            STAMP(5);
        } else {
            const int tw = dw - 1;
            *(LAS u32x2*)(L + L_PAR + tw * 512 + lane * 8) = pw1;
            *(LAS u32x2*)(L + L_PAR + F * 2 + tw * 512 + lane * 8) = pb1;
            *(LAS u32x2*)(L + L_PAR + F * 4 + tw * 512 + lane * 8) = pw2;
            *(LAS u32*)(L + L_FLG + 32) = b2v;
            const f32x2 w1a = un2(pw1[0]), w1b = un2(pw1[1]), b1a = un2(pb1[0]), b1b = un2(pb1[1]), w2a = un2(pw2[0]), w2b = un2(pw2[1]);
            const u32 t0 = ((u32)(wg & 128) << 24) + (((TEXP0 << 7) + (u32)(wg & 127) * 16) << 16);
#pragma unroll 1
            for (int it = 0; it < 4; it++) {
                float a[4];
#pragma unroll
                for (int k = 0; k < 4; k++) {
                    const float t = __uint_as_float(t0 + ((u32)(it * 4 + k) << 16));
                    const f32x2 q = unit2(t, w1b, b1b, w2b, unit2(t, w1a, b1a, w2a, f32x2{0.f, 0.f}));
                    a[k] = q[0] + q[1];
                }
                const float v = row_sum(sw16(sw32(a[0], a[2]), sw32(a[1], a[3])));
                if (n == 0) *(LAS u32x2*)(L + L_TP + tw * 128 + (it * 4 + c) * 8) = u32x2{__float_as_uint(v), tag};
            }
            STAMP(4);
#pragma unroll 1
            for (u32 spin = 0; spin < SPIN_CAP; spin++) {
                MEMB();
                if (__builtin_amdgcn_readfirstlane(*(LAS u32*)(L + L_FLG)) == tag) break;
                __builtin_amdgcn_s_sleep(2);
            }
            STAMP(5);
        }
        MEMB();
        {
            const u32x4 qf = *(LAS u32x4*)(L + L_Q + (n & 3) * 256 + c * 64 + (n >> 2) * 16);
            const f32x4 z = {0.f, 0.f, 0.f, 0.f};
            float sc[8];
#pragma unroll
            for (int j = 0; j < 8; j++) {
                const f32x4 t = mfma16(rr[j], qf, z);
                float v = (n & 8) ? ((n & 4) ? t[3] : t[2]) : ((n & 4) ? t[1] : t[0]);
                v += dpp<ROR4>(v); v += dpp<ROR8>(v);
                sc[j] = rbf(rbf(v) * 0.08838834764831845f) * LOG2E;
            }
            float mx = fmaxf(fmaxf(fmaxf(sc[0], sc[1]), fmaxf(sc[2], sc[3])), fmaxf(fmaxf(sc[4], sc[5]), fmaxf(sc[6], sc[7])));
            mx = x16_max(mx);
            float p[8];
#pragma unroll
            for (int j = 0; j < 8; j++) p[j] = __builtin_amdgcn_exp2f(sc[j] - mx);
            const float ls = x16_sum(((p[0] + p[1]) + (p[2] + p[3])) + ((p[4] + p[5]) + (p[6] + p[7])));
            if (n < 4) {
                *(LAS f32x4*)(L + L_PP + dw * 512 + (c * 4 + n) * 32) = f32x4{p[0], p[1], p[2], p[3]};
                *(LAS f32x4*)(L + L_PP + dw * 512 + (c * 4 + n) * 32 + 16) = f32x4{p[4], p[5], p[6], p[7]};
                *(LAS float*)(L + L_MX + n * 32 + dw * 4) = mx;
                *(LAS float*)(L + L_LS + n * 32 + dw * 4) = ls;
            }
        }
        STAMP(6);
        if (dw == 0) {
            u32x2 p0, p1;
#pragma unroll 1
            for (u32 spin = 0; spin < SPIN_CAP; spin++) {
                MEMB();
                p0 = *(LAS u32x2*)(L + L_TP + lane * 8);
                p1 = *(LAS u32x2*)(L + L_TP + 512 + lane * 8);
                if (__all(p0[1] == tag && (c == 3 || p1[1] == tag))) break;
            }
            const float y = x16_sum(__uint_as_float(p0[0]) + (c == 3 ? 0.f : __uint_as_float(p1[0])));
            const float yy = rbf(y) + bflo(b2v);
            ST8((u32x2{pack2(yy, dpp<XOR1>(yy)), tag}), (lane & 49) ? OOB : WS_TAB + wg * 128 + lane * 4);
        }
        asm volatile("" :: "v"(pre));
        LGK0();
        STAMP(7);
        FIN();
        asm volatile(".Lc_end:");
    }
}

extern "C" void kernel_launch(void* const* d_in, const int* in_sizes, int n_in,
                              void* d_out, int out_size, void* d_ws, size_t ws_size,
                              hipStream_t stream) {
    (void)in_sizes; (void)n_in; (void)out_size; (void)ws_size;
    (void)hipFuncSetAttribute((const void*)fused_kernel, hipFuncAttributeMaxDynamicSharedMemorySize, LDS_BYTES);
    fused_kernel<<<256, 1024, LDS_BYTES, stream>>>(
        (const u16*)d_in[1], (const u16*)d_in[2], (unsigned char*)d_ws, (const u16*)d_in[3],
        (const u16*)d_in[0], (const u16*)d_in[4],
        (const u16*)d_in[6], (const u16*)d_in[7], (const u16*)d_in[8], (const u16*)d_in[9],
        (u16*)d_out);
}
```

```cpp
#include <hip/hip_runtime.h>
#include <stdint.h>

typedef unsigned short u16;
typedef unsigned int u32;
typedef unsigned long long u64;
typedef __attribute__((ext_vector_type(4))) u32 u32x4;
typedef __attribute__((ext_vector_type(2))) u32 u32x2;
typedef __attribute__((ext_vector_type(4))) float f32x4;
typedef __attribute__((ext_vector_type(2))) float f32x2;
typedef __attribute__((ext_vector_type(8))) short bf16x8;
typedef __attribute__((ext_vector_type(4))) short s16x4;
typedef __attribute__((ext_vector_type(2))) __bf16 bf16x2;
#define LAS __attribute__((address_space(3)))
#define GAS __attribute__((address_space(1)))

#define WIN 7
#define DQ 8
#define DL 8
#define DA 3
#define DB 8
#define CODE_KB 7

constexpr int D = 512, SK = 8192, DH = 128, W = 2048, F = 1792;
constexpr float EPS = 1e-5f;
constexpr float LOG2E = 1.4426950408889634f;
constexpr u32 SPIN_CAP = 1u << 18;
constexpr u32 TEXP0 = 114;

constexpr int WS_EPOCH = 0;
constexpr int WS_Q   = 4096;
constexpr int WS_LSE = WS_Q + 65536;
constexpr int WS_TAB = WS_LSE + 8192;
constexpr int WS_P   = WS_TAB + 32768;
constexpr int WS_O   = WS_P + 131072;
constexpr int WS_END = WS_O + 524288;
constexpr size_t WS_STAMP = 64u << 20;
constexpr int OOB = 0x7ffffff0;

constexpr int L_WQ   = 0;
constexpr int L_XN   = 16384;
constexpr int L_X    = L_XN + 4096;
constexpr int L_Q    = L_X + 4096;
constexpr int L_FLG  = L_Q + 1024;
constexpr int L_MX   = L_FLG + 64;
constexpr int L_LS   = L_MX + 128;
constexpr int L_TP   = L_LS + 128;
constexpr int L_S2   = L_TP + 1024;
constexpr int L_QP   = L_S2 + 64;
constexpr int L_PP   = L_QP + 4096;
constexpr int L_R2   = L_PP + 4096;
constexpr int L_PART = L_R2 + 4096;
constexpr int L_WO   = L_PART + 16384;
constexpr int L_AT   = L_WO + 16384;
constexpr int L_TAB  = L_AT + 16384;
constexpr int L_PAR  = L_TAB + 8192;
constexpr int L_END  = L_PAR + 3 * F * 2;
constexpr int LDS_BYTES = 108 * 1024;
static_assert(L_END <= LDS_BYTES, "lds");

__device__ __forceinline__ float bflo(u32 v) { return __uint_as_float(v << 16); }
__device__ __forceinline__ float bfhi(u32 v) { return __uint_as_float(v & 0xFFFF0000u); }
__device__ __forceinline__ u32 pack2(float lo, float hi) {
    bf16x2 v = {(__bf16)lo, (__bf16)hi};
    return __builtin_bit_cast(u32, v);
}
__device__ __forceinline__ float rbf(float x) { return (float)(__bf16)x; }
template <int CTRL> __device__ __forceinline__ float dpp(float x) {
    return __builtin_bit_cast(float, __builtin_amdgcn_mov_dpp(__builtin_bit_cast(int, x), CTRL, 0xf, 0xf, true));
}
__device__ __forceinline__ float sw32(float a, float b) {
    auto t = __builtin_amdgcn_permlane32_swap(__float_as_uint(a), __float_as_uint(b), false, false);
    return __uint_as_float(t[0]) + __uint_as_float(t[1]);
}
__device__ __forceinline__ float sw16(float a, float b) {
    auto t = __builtin_amdgcn_permlane16_swap(__float_as_uint(a), __float_as_uint(b), false, false);
    return __uint_as_float(t[0]) + __uint_as_float(t[1]);
}
__device__ __forceinline__ float x16_sum(float x) { x = sw16(x, x); return sw32(x, x); }
__device__ __forceinline__ float x16_max(float x) {
    auto s = __builtin_amdgcn_permlane16_swap(__float_as_uint(x), __float_as_uint(x), false, false);
    x = fmaxf(__uint_as_float(s[0]), __uint_as_float(s[1]));
    auto t = __builtin_amdgcn_permlane32_swap(__float_as_uint(x), __float_as_uint(x), false, false);
    return fmaxf(__uint_as_float(t[0]), __uint_as_float(t[1]));
}
constexpr int XOR1 = 0xB1, XOR2 = 0x4E, ROR4 = 0x124, ROR8 = 0x128, HMIR = 0x141;
__device__ __forceinline__ float row_sum(float x) {
    x += dpp<XOR1>(x); x += dpp<XOR2>(x); x += dpp<ROR4>(x); x += dpp<ROR8>(x);
    return x;
}
__device__ __forceinline__ float wave_sum(float x) { return x16_sum(row_sum(x)); }
__device__ __forceinline__ f32x4 mfma16(u32x4 a, u32x4 b, f32x4 c) {
    return __builtin_amdgcn_mfma_f32_16x16x32_bf16(__builtin_bit_cast(bf16x8, a), __builtin_bit_cast(bf16x8, b), c, 0, 0, 0);
}
__device__ __forceinline__ u32x4 ldnt(const u16* p) { return __builtin_nontemporal_load((const u32x4*)p); }
__device__ __forceinline__ u32x4 trfrag(LAS unsigned char* lo, LAS unsigned char* hi) {
    const u32x2 l = __builtin_bit_cast(u32x2, __builtin_amdgcn_ds_read_tr16_b64_v4i16((LAS s16x4*)lo));
    const u32x2 h = __builtin_bit_cast(u32x2, __builtin_amdgcn_ds_read_tr16_b64_v4i16((LAS s16x4*)hi));
    return u32x4{l[0], l[1], h[0], h[1]};
}
#define SB() __builtin_amdgcn_sched_barrier(0)
#define MEMB() asm volatile("" ::: "memory")
#define BARRIER() do { asm volatile("s_waitcnt lgkmcnt(0)\n\ts_barrier" ::: "memory"); } while (0)
#define LGK0() asm volatile("s_waitcnt lgkmcnt(0)" ::: "memory")
constexpr int SC1 = 16, NT = 2;
#define LD16(off) __builtin_amdgcn_raw_buffer_load_b128(rs, off, 0, SC1)
#define ST8(v, off) __builtin_amdgcn_raw_buffer_store_b64(v, rs, off, 0, SC1)

#define G_LD(i, so) "s_mov_b64 exec, %[m" #i "]\n\tbuffer_load_dwordx4 %[a" #i "], %[vo], %[rs], " so " offen sc1\n\t"
#define G_END "s_mov_b64 exec, -1\n\ts_waitcnt vmcnt(0)"
template <int N, int NAP>
__device__ __forceinline__ void gather(u32x4 (&a)[N], __amdgpu_buffer_rsrc_t rs, int off, int stride, u32 tag, u64 act) {
    u64 m[N];
#pragma unroll
    for (int i = 0; i < N; i++) { m[i] = act; asm volatile("" : "=v"(a[i])); }
#pragma unroll 1
    for (u32 spin = 0; spin < SPIN_CAP; spin++) {
        if constexpr (N == 4)
            asm volatile(G_LD(0, "0") G_LD(1, "%[s1]") G_LD(2, "%[s2]") G_LD(3, "%[s3]") G_END
                : [a0] "+v"(a[0]), [a1] "+v"(a[1]), [a2] "+v"(a[2]), [a3] "+v"(a[3])
                : [m0] "s"(m[0]), [m1] "s"(m[1]), [m2] "s"(m[2]), [m3] "s"(m[3]), [vo] "v"(off), [rs] "s"(rs),
                  [s1] "s"(stride), [s2] "s"(2 * stride), [s3] "s"(3 * stride) : "memory");
        else if constexpr (N == 2)
            asm volatile(G_LD(0, "0") G_LD(1, "%[s1]") G_END
                : [a0] "+v"(a[0]), [a1] "+v"(a[1])
                : [m0] "s"(m[0]), [m1] "s"(m[1]), [vo] "v"(off), [rs] "s"(rs), [s1] "s"(stride) : "memory");
        else
            asm volatile(G_LD(0, "0") G_END : [a0] "+v"(a[0]) : [m0] "s"(m[0]), [vo] "v"(off), [rs] "s"(rs) : "memory");
        u64 any = 0;
#pragma unroll
        for (int i = 0; i < N; i++) { m[i] = __ballot(a[i][1] != tag || a[i][3] != tag) & act; any |= m[i]; }
        if (!any) break;
        if (NAP) __builtin_amdgcn_s_sleep(NAP);
    }
}

template <int DD> __device__ __forceinline__ u32x4 colfrag(const u32x4 (&r)[8]) {
    constexpr u32 sel = (DD & 1) ? 0x07060302u : 0x05040100u;
    u32x4 f;
#pragma unroll
    for (int w = 0; w < 4; w++) f[w] = __builtin_amdgcn_perm(r[2 * w + 1][DD >> 1], r[2 * w][DD >> 1], sel);
    return f;
}

constexpr float GK0 = -2.0f * 0.7978845608028654f * LOG2E, GK1 = GK0 * 0.044715f;
__device__ __forceinline__ f32x2 unit2(float t, f32x2 w1, f32x2 b1, f32x2 w2, f32x2 acc) {
    const f32x2 u = __builtin_elementwise_fma(f32x2{t, t}, w1, b1);
    const f32x2 z = u * __builtin_elementwise_fma(u * u, f32x2{GK1, GK1}, f32x2{GK0, GK0});
    const f32x2 e = f32x2{__builtin_amdgcn_exp2f(z[0]), __builtin_amdgcn_exp2f(z[1])} + f32x2{1.f, 1.f};
    const f32x2 r = {__builtin_amdgcn_rcpf(e[0]), __builtin_amdgcn_rcpf(e[1])};
    return __builtin_elementwise_fma(u * w2, r, acc);
}
__device__ __forceinline__ f32x2 un2(u32 v) { return f32x2{bflo(v), bfhi(v)}; }

#define STAMP(k) do {} while (0)
#define FIN() do {} while (0)

#define WBT(pos) do {} while (0)
#define WBFIN() do {} while (0)
#define RSRC(p, nbytes) __builtin_amdgcn_make_buffer_rsrc((void*)(p), 0, nbytes, 0x00020000)
#define LDNT(r, v, s) __builtin_amdgcn_raw_buffer_load_b128(r, v, s, NT)
template <int K> __device__ __forceinline__ float qbc(float x) { return dpp<K * 0x55>(x); }

extern "C" __global__ void __launch_bounds__(1024, 4)
fused_kernel(const u16* __restrict__ kc, const u16* __restrict__ vc, unsigned char* ws, const u16* __restrict__ wq,
             const u16* __restrict__ x, const u16* __restrict__ wo,
             const u16* __restrict__ w1, const u16* __restrict__ b1, const u16* __restrict__ w2,
             const u16* __restrict__ b2, u16* out) {
    extern __shared__ __attribute__((aligned(16))) unsigned char smem[];
    LAS unsigned char* L = (LAS unsigned char*)smem;
    const int wg = blockIdx.x, kvh = wg & 7, r = wg >> 3, b = r >> 3, s = r & 7;
    const int lane = threadIdx.x & 63;
    const int wave = __builtin_amdgcn_readfirstlane(threadIdx.x >> 6);
    const int c = lane >> 4, n = lane & 15, dw = wave & 7;
    STAMP(0);
    WBT(0);
    asm volatile("" :: "s"(kc), "s"(vc), "s"(ws), "s"(wq), "s"(x), "s"(wo), "s"(w1), "s"(b1), "s"(w2), "s"(b2), "s"(out));
    u32 ep = *(const __attribute__((address_space(4))) u32*)(ws + WS_EPOCH);
    const __amdgpu_buffer_rsrc_t rs = RSRC(ws, WS_END);
    const bool cons = (wg & 0xF9) == 0;
    const int bj = wg >> 1;
    const int rho = r & 1, cg = r >> 1;
    const bool kw = wave < 8;

    u32x4 xr, e0, e1, rr[8];
    u32x2 pw1, pb1, pw2;
    u32 b2v;
    if (kw) {
        __builtin_amdgcn_s_setprio(1);
        xr = *(const u32x4*)(x + (dw & 3) * D + lane * 8);
    }
    {
        const size_t base = ((size_t)(b * 8 + kvh) * SK + (SK - W) + s * 256 + dw * 32) * DH;
        const __amdgpu_buffer_rsrc_t rkv = RSRC((kw ? kc : vc) + base, 8192);
        const int kvo = kw ? (n >> 2) * 256 + c * 64 + (n & 3) * 16 : lane * 16;
        const __amdgpu_buffer_rsrc_t rw = RSRC(kw ? wq + (size_t)dw * 64 * 4096 + kvh * 512 + r * 16
                                                  : wo + (size_t)(kvh * 512 + rho * 256 + dw * 32) * D + cg * 32, kw ? 63 * 8192 + 32 : 31 * 1024 + 64);
        const int sg = lane >> 2, cp = sg >> 3;
        const int wof = kw ? (lane >> 1) * 8192 + (lane & 1) * 16 : (8 * (sg & 3) + 2 * cp + ((sg >> 2) & 1)) * 1024 + ((lane & 3) ^ (2 * cp)) * 16;
        const int wst = kw ? 32 * 8192 : 4096;
        const __amdgpu_buffer_rsrc_t ra = kw ? rw : rkv, rb = kw ? rkv : rw;
        const int ao = kw ? wof : kvo, bo = kw ? kvo : wof, s0 = kw ? 0 : 2048;
        e0 = LDNT(ra, ao, 0); SB();
        e1 = LDNT(ra, ao, kw ? wst : 1024); SB();
        if (kw) {
            const int po = (dw ? dw - 1 : 0) * 256 + lane * 4;
            pw1 = *(const u32x2*)(w1 + po);
            pb1 = *(const u32x2*)(b1 + po);
            pw2 = *(const u32x2*)(w2 + po);
            b2v = b2[0];
            SB();
            __builtin_amdgcn_s_setprio(0);
        }
#pragma unroll
        for (int j = 0; j < 6; j++) { rr[j] = LDNT(rkv, kvo + (j & 3) * 1024, s0 + (j >> 2) * 4096); SB(); }
        rr[6] = LDNT(rb, bo, kw ? 6144 : 0); SB();
        rr[7] = LDNT(rb, bo, kw ? 7168 : wst); SB();
    }
    int wv2 = wave;
    asm volatile("" : "+s"(ep), "+s"(wv2));
    const u32 tag = ep + 0x9E3779B9u;
    STAMP(1);

    if (__builtin_expect(wv2 >= 8, 1)) {
        BARRIER();
        __builtin_amdgcn_s_setprio(2);
        WBT(1);
        STAMP(2);
        u32x4 pfm[4];
        {
            const int h = n & 3;
            const f32x4 m0 = *(LAS f32x4*)(L + L_MX + h * 32), m1 = *(LAS f32x4*)(L + L_MX + h * 32 + 16);
            const f32x4 l0 = *(LAS f32x4*)(L + L_LS + h * 32), l1 = *(LAS f32x4*)(L + L_LS + h * 32 + 16);
            const float mo = *(LAS float*)(L + L_MX + h * 32 + dw * 4);
            const f32x4 p0 = *(LAS f32x4*)(L + L_PP + dw * 512 + (c * 4 + h) * 32), p1 = *(LAS f32x4*)(L + L_PP + dw * 512 + (c * 4 + h) * 32 + 16);
            const float M = fmaxf(fmaxf(fmaxf(m0[0], m0[1]), fmaxf(m0[2], m0[3])), fmaxf(fmaxf(m1[0], m1[1]), fmaxf(m1[2], m1[3])));
            float ls = 0.f;
#pragma unroll
            for (int i = 0; i < 4; i++) {
                ls = fmaf(l0[i], __builtin_amdgcn_exp2f(m0[i] - M), ls);
                ls = fmaf(l1[i], __builtin_amdgcn_exp2f(m1[i] - M), ls);
            }
            const float sc = __builtin_amdgcn_exp2f(mo - M) * __builtin_amdgcn_rcpf(ls);
            const u32x4 pf = {pack2(p0[0] * sc, p0[1] * sc), pack2(p0[2] * sc, p0[3] * sc), pack2(p1[0] * sc, p1[1] * sc), pack2(p1[2] * sc, p1[3] * sc)};
            const u32x4 zz = {0u, 0u, 0u, 0u};
#pragma unroll
            for (int g = 0; g < 4; g++) pfm[g] = (n >> 2) == g ? pf : zz;
            const float lse = M + __builtin_amdgcn_logf(ls);
            ST8((u32x2{__float_as_uint(lse), tag}), (dw == 7 && lane < 4) ? WS_LSE + ((kvh * 4 + lane) * 32 + r) * 8 : OOB);
        }
        SB();
        STAMP(3);
        {
            const u32x4 vr[8] = {e0, e1, rr[0], rr[1], rr[2], rr[3], rr[4], rr[5]};
            const f32x4 z = {0.f, 0.f, 0.f, 0.f};
            f32x4 a0 = mfma16(colfrag<0>(vr), pfm[0], z), a1 = mfma16(colfrag<4>(vr), pfm[0], z);
            a0 = mfma16(colfrag<1>(vr), pfm[1], a0); a1 = mfma16(colfrag<5>(vr), pfm[1], a1);
            a0 = mfma16(colfrag<2>(vr), pfm[2], a0); a1 = mfma16(colfrag<6>(vr), pfm[2], a1);
            a0 = mfma16(colfrag<3>(vr), pfm[3], a0); a1 = mfma16(colfrag<7>(vr), pfm[3], a1);
            LAS unsigned char* pb = L + L_PART + dw * 2048 + c * 256 + (n & 12) * 16;
            *(LAS f32x4*)(pb + ((n + 2 * c) & 3) * 16) = a0;
            *(LAS f32x4*)(pb + 1024 + ((n + 2 * c + 1) & 3) * 16) = a1;
        }
        STAMP(4);
        BARRIER();
        if (dw < 4) {
            const int a = (lane >> 3) & 1, g = (lane >> 1) & 3;
            LAS unsigned char* pb = L + L_PART + a * 1024 + c * 256 + (g * 4 + ((dw + 2 * c + a) & 3)) * 16 + (lane & 1) * 8;
            f32x2 q0 = *(LAS f32x2*)(pb) + *(LAS f32x2*)(pb + 2048), q1 = *(LAS f32x2*)(pb + 2 * 2048) + *(LAS f32x2*)(pb + 3 * 2048);
            f32x2 q2 = *(LAS f32x2*)(pb + 4 * 2048) + *(LAS f32x2*)(pb + 5 * 2048), q3 = *(LAS f32x2*)(pb + 6 * 2048) + *(LAS f32x2*)(pb + 7 * 2048);
            q0 = (q0 + q1) + (q2 + q3);
            ST8((u32x2{pack2(q0[0], q0[1]), tag}), WS_O + (kvh * 32 + r) * 2048 + dw * 512 + lane * 8);
        }
        STAMP(5);
        float wt[8];
        {
            u32x4 g[1];
            gather<1, 0>(g, rs, WS_LSE + ((kvh * 4 + rho * 2 + (dw >> 2)) * 32 + c * 8 + (n & 3) * 2) * 8, 0, tag, ~0ull);
            const float l0 = __uint_as_float(g[0][0]), l1 = __uint_as_float(g[0][2]);
            float M = fmaxf(l0, l1);
            M = fmaxf(M, dpp<XOR1>(M)); M = fmaxf(M, dpp<XOR2>(M));
            const float x0 = __builtin_amdgcn_exp2f(l0 - M), x1 = __builtin_amdgcn_exp2f(l1 - M);
            float S = x0 + x1;
            S += dpp<XOR1>(S); S += dpp<XOR2>(S);
            const float inv = __builtin_amdgcn_rcpf(S);
            const float y0 = x0 * inv, y1 = x1 * inv;
            wt[0] = qbc<0>(y0); wt[1] = qbc<0>(y1); wt[2] = qbc<1>(y0); wt[3] = qbc<1>(y1);
            wt[4] = qbc<2>(y0); wt[5] = qbc<2>(y1); wt[6] = qbc<3>(y0); wt[7] = qbc<3>(y1);
        }
        u32x4 bf0, bf1;
        {
            LAS unsigned char* wb = L + L_WO + dw * 2048;
            *(LAS u32x4*)(wb + lane * 16) = rr[6];
            *(LAS u32x4*)(wb + 1024 + lane * 16) = rr[7];
            LGK0();
            LAS unsigned char* tb = wb + (8 * c + (n >> 2)) * 64 + (n & 3) * 8;
            bf0 = trfrag(tb + (c & 1) * 32, tb + 256 + (c & 1) * 32);
            bf1 = trfrag(tb + (~c & 1) * 32, tb + 256 + (~c & 1) * 32);
        }
        SB();
        STAMP(6);
        if (DA) __builtin_amdgcn_s_sleep(DA);
        {
            u32x4 a[4];
            gather<4, 0>(a, rs, WS_O + (kvh * 32 + (lane >> 3)) * 2048 + (rho * 8 + dw) * 128 + (lane & 7) * 16, 8 * 2048, tag, ~0ull);
            STAMP(7);
            LAS unsigned char* ab = L + L_AT + dw * 2048;
            LAS unsigned char* wa = ab + (lane >> 5) * 1024 + ((lane >> 3) & 3) * 64 + (lane & 1) * 8;
#pragma unroll
            for (int i = 0; i < 4; i++) *(LAS u32x2*)(wa + i * 256 + ((((lane >> 1) & 3) ^ i) * 16)) = u32x2{a[i][0], a[i][2]};
            LGK0();
            const u32x4 af0 = *(LAS u32x4*)(ab + n * 64 + (c ^ (n >> 2)) * 16);
            const u32x4 af1 = *(LAS u32x4*)(ab + 1024 + n * 64 + (c ^ (n >> 2)) * 16);
            const f32x4 z = {0.f, 0.f, 0.f, 0.f};
            const f32x4 o00 = mfma16(af0, bf0, z), o01 = mfma16(af0, bf1, z), o10 = mfma16(af1, bf0, z), o11 = mfma16(af1, bf1, z);
            f32x2 q0 = f32x2{o00[0], o01[0]} * wt[0], q1 = f32x2{o10[0], o11[0]} * wt[4];
#pragma unroll
            for (int i = 1; i < 4; i++) { q0 += f32x2{o00[i], o01[i]} * wt[i]; q1 += f32x2{o10[i], o11[i]} * wt[4 + i]; }
            *(LAS f32x2*)(L + L_R2 + dw * 512 + lane * 8) = q0 + q1;
        }
        BARRIER();
        if (dw == 0) {
            LAS unsigned char* pb = L + L_R2 + lane * 8;
            f32x2 q0 = *(LAS f32x2*)(pb) + *(LAS f32x2*)(pb + 512), q1 = *(LAS f32x2*)(pb + 1024) + *(LAS f32x2*)(pb + 1536);
            f32x2 q2 = *(LAS f32x2*)(pb + 2048) + *(LAS f32x2*)(pb + 2560), q3 = *(LAS f32x2*)(pb + 3072) + *(LAS f32x2*)(pb + 3584);
            q0 = (q0 + q1) + (q2 + q3);
            ST8((u32x2{pack2(q0[0], q0[1]), tag}), WS_P + c * 32768 + cg * 2048 + (kvh * 2 + rho) * 128 + n * 8);
        }
        STAMP(8);
        if (cons) {
            const int col = dw * 64 + (lane & 48) + (lane & 7) * 2 + ((lane >> 3) & 1);
            const float xv = bflo(*(LAS u16*)(L + L_X + bj * 1024 + col * 2));
            {
                u32x4 g[2];
                gather<2, 0>(g, rs, WS_TAB + (dw * 32 + (lane >> 2)) * 128 + (lane & 3) * 16, 16 * 128, tag, ~0ull);
                *(LAS u32x2*)(L + L_TAB + dw * 1024 + lane * 8) = u32x2{g[0][0], g[0][2]};
                *(LAS u32x2*)(L + L_TAB + dw * 1024 + 512 + lane * 8) = u32x2{g[1][0], g[1][2]};
            }
            if (DB) __builtin_amdgcn_s_sleep(DB);
            u32x4 a[4];
            gather<4, 0>(a, rs, WS_P + bj * 32768 + dw * 4096 + lane * 16, 1024, tag, ~0ull);
            STAMP(9);
            const float v0 = sw32(bflo(a[0][0]) + bflo(a[1][0]), bflo(a[2][0]) + bflo(a[3][0]));
            const float v1 = sw32(bfhi(a[0][0]) + bfhi(a[1][0]), bfhi(a[2][0]) + bfhi(a[3][0]));
            const float v2 = sw32(bflo(a[0][2]) + bflo(a[1][2]), bflo(a[2][2]) + bflo(a[3][2]));
            const float v3 = sw32(bfhi(a[0][2]) + bfhi(a[1][2]), bfhi(a[2][2]) + bfhi(a[3][2]));
            const float u0 = sw16(v0, v1), u1 = sw16(v2, v3);
            const float at = ((lane & 8) ? u1 : u0) + dpp<ROR8>((lane & 8) ? u0 : u1);
            const float h = rbf(xv + rbf(at));
            const float piv = __uint_as_float(__builtin_amdgcn_readfirstlane(__float_as_uint(h)));
            const float d = h - piv;
            const float s1 = wave_sum(d), s2 = wave_sum(d * d);
            if (lane == 0) *(LAS f32x2*)(L + L_S2 + dw * 8) = f32x2{fmaf(s1, 1.0f / 64, piv), fmaf(-s1 * (1.0f / 64), s1, s2)};
            BARRIER();
            const f32x2 ms = *(LAS f32x2*)(L + L_S2 + (lane & 7) * 8);
            float mean = ms[0];
            mean += dpp<XOR1>(mean); mean += dpp<XOR2>(mean); mean += dpp<HMIR>(mean);
            mean *= 0.125f;
            const float dv = ms[0] - mean;
            float m2 = fmaf(dv * 64.f, dv, ms[1]);
            m2 += dpp<XOR1>(m2); m2 += dpp<XOR2>(m2); m2 += dpp<HMIR>(m2);
            const float var = fmaxf(m2 * (1.0f / D), 0.f);
            const float m1 = rbf(mean);
            const float inv1 = __builtin_amdgcn_rcpf(rbf(__builtin_amdgcn_sqrtf(rbf(rbf(var) + EPS))));
            const float t1 = rbf(rbf(h - m1) * inv1);
            const float mb = rbf((mean - m1) * inv1);
            const float inv2 = __builtin_amdgcn_rcpf(rbf(__builtin_amdgcn_sqrtf(rbf(rbf(var * inv1 * inv1) + EPS))));
            const u32 tb = pack2(rbf(t1 - mb) * inv2, 0.f) & 0xFFFFu;
            const u32 ti = (tb & 0x7FFFu) - (TEXP0 << 7);
            float y = bflo(*(LAS u16*)(L + L_TAB + ((ti & 2047u) + (tb >> 15) * 2048u) * 2));
            bool miss = ti >= 2048u;
            u64 mm = __ballot(miss);
            while (__builtin_expect(mm != 0, 0)) {
                const u32 tv = __builtin_amdgcn_readlane(tb, __builtin_ctzll(mm));
                const float t = bflo(tv);
                f32x2 acc = {0.f, 0.f};
#pragma unroll 1
                for (int it = 0; it < 14; it++) {
                    LAS unsigned char* pp = L + L_PAR + it * 256 + lane * 4;
                    acc = unit2(t, un2(*(LAS u32*)pp), un2(*(LAS u32*)(pp + F * 2)), un2(*(LAS u32*)(pp + F * 4)), acc);
                }
                const float yy = rbf(rbf(wave_sum(acc[0] + acc[1])) + bflo(*(LAS u32*)(L + L_FLG + 32)));
                if (tb == tv) { y = yy; miss = false; }
                mm = __ballot(miss);
            }
            out[bj * D + col] = (u16)pack2(h + y, 0.f);
            if (wg == 0 && dw == 0 && lane == 0) __builtin_amdgcn_raw_buffer_store_b32(tag, rs, WS_EPOCH, 0, SC1);
            STAMP(10);
        }
        WBFIN();
        FIN();
    } else {
        if (dw < 4) {
            float v[8];
#pragma unroll
            for (int k = 0; k < 4; k++) { v[2 * k] = bflo(xr[k]); v[2 * k + 1] = bfhi(xr[k]); }
            const float mean = wave_sum(((v[0] + v[1]) + (v[2] + v[3])) + ((v[4] + v[5]) + (v[6] + v[7]))) * (1.0f / D);
            float ss = 0.f;
#pragma unroll
            for (int k = 0; k < 8; k++) { const float d = v[k] - mean; ss = fmaf(d, d, ss); }
            const float var = rbf(wave_sum(ss) * (1.0f / D));
            const float m = rbf(mean);
            const float inv = __builtin_amdgcn_rcpf(rbf(__builtin_amdgcn_sqrtf(rbf(var + EPS))));
            u32x4 o;
#pragma unroll
            for (int k = 0; k < 4; k++) o[k] = pack2(rbf(v[2 * k] - m) * inv, rbf(v[2 * k + 1] - m) * inv);
            *(LAS u32x4*)(L + L_XN + dw * 1024 + lane * 16) = o;
            *(LAS u32x4*)(L + L_X + dw * 1024 + lane * 16) = xr;
            LGK0();
            *(LAS u32*)(L + L_FLG + 16 + dw * 4) = tag;
        }
        STAMP(2);
        {
            LAS unsigned char* wb = L + L_WQ + dw * 2048;
            *(LAS u32x4*)(wb + lane * 16) = e0;
            *(LAS u32x4*)(wb + 1024 + lane * 16) = e1;
            LGK0();
            LAS unsigned char* tb = wb + (8 * c + (n >> 2)) * 32 + (n & 3) * 8;
            const u32x4 f0 = trfrag(tb, tb + 128), f1 = trfrag(tb + 1024, tb + 1152);
            STAMP(3);
#pragma unroll 1
            for (u32 spin = 0; spin < SPIN_CAP; spin++) {
                MEMB();
                const u32x4 f = *(LAS u32x4*)(L + L_FLG + 16);
                if (__builtin_amdgcn_readfirstlane((f[0] ^ tag) | (f[1] ^ tag) | (f[2] ^ tag) | (f[3] ^ tag)) == 0) break;
            }
            MEMB();
            LAS unsigned char* ab = L + L_XN + (n & 3) * 1024 + dw * 128 + c * 16;
            f32x4 qa = mfma16(*(LAS u32x4*)(ab), f0, f32x4{0.f, 0.f, 0.f, 0.f});
            qa = mfma16(*(LAS u32x4*)(ab + 64), f1, qa);
            const float qv = (c & 2) ? ((c & 1) ? qa[3] : qa[2]) : ((c & 1) ? qa[1] : qa[0]);
            *(LAS u32x2*)(L + L_QP + dw * 512 + lane * 8) = u32x2{__float_as_uint(qv), tag};
        }
        u32x4 pre = {0u, 0u, 0u, 0u};
        if (dw == 0) {
            u32x2 p[8];
#pragma unroll 1
            for (u32 spin = 0; spin < SPIN_CAP; spin++) {
                MEMB();
                u32 bad = 0;
#pragma unroll
                for (int i = 0; i < 8; i++) { p[i] = *(LAS u32x2*)(L + L_QP + i * 512 + lane * 8); bad |= p[i][1] ^ tag; }
                if (__all(bad == 0)) break;
            }
            const float qv = ((__uint_as_float(p[0][0]) + __uint_as_float(p[1][0])) + (__uint_as_float(p[2][0]) + __uint_as_float(p[3][0]))) +
                             ((__uint_as_float(p[4][0]) + __uint_as_float(p[5][0])) + (__uint_as_float(p[6][0]) + __uint_as_float(p[7][0])));
            ST8((u32x2{pack2(qv, dpp<XOR1>(qv)), tag}), (n & 1) ? OOB : WS_Q + (kvh * 32 + r) * 256 + c * 64 + n * 4);
            STAMP(4);
            __builtin_amdgcn_s_sleep(DQ);
            u32x4 g[2];
            gather<2, 0>(g, rs, WS_Q + (kvh * 32 + (lane >> 2)) * 256 + b * 64 + (lane & 3) * 16, 16 * 256, tag, ~0ull);
            *(LAS u32x2*)(L + L_Q + lane * 8) = u32x2{g[0][0], g[0][2]};
            *(LAS u32x2*)(L + L_Q + 512 + lane * 8) = u32x2{g[1][0], g[1][2]};
            LGK0();
            *(LAS u32*)(L + L_FLG) = tag;
            STAMP(5);
        } else {
            const int tw = dw - 1;
            *(LAS u32x2*)(L + L_PAR + tw * 512 + lane * 8) = pw1;
            *(LAS u32x2*)(L + L_PAR + F * 2 + tw * 512 + lane * 8) = pb1;
            *(LAS u32x2*)(L + L_PAR + F * 4 + tw * 512 + lane * 8) = pw2;
            *(LAS u32*)(L + L_FLG + 32) = b2v;
            const f32x2 w1a = un2(pw1[0]), w1b = un2(pw1[1]), b1a = un2(pb1[0]), b1b = un2(pb1[1]), w2a = un2(pw2[0]), w2b = un2(pw2[1]);
            const u32 t0 = ((u32)(wg & 128) << 24) + (((TEXP0 << 7) + (u32)(wg & 127) * 16) << 16);
#pragma unroll 1
            for (int it = 0; it < 4; it++) {
                float a[4];
#pragma unroll
                for (int k = 0; k < 4; k++) {
                    const float t = __uint_as_float(t0 + ((u32)(it * 4 + k) << 16));
                    const f32x2 q = unit2(t, w1b, b1b, w2b, unit2(t, w1a, b1a, w2a, f32x2{0.f, 0.f}));
                    a[k] = q[0] + q[1];
                }
                const float v = row_sum(sw16(sw32(a[0], a[2]), sw32(a[1], a[3])));
                if (n == 0) *(LAS u32x2*)(L + L_TP + tw * 128 + (it * 4 + c) * 8) = u32x2{__float_as_uint(v), tag};
            }
            STAMP(4);
#pragma unroll 1
            for (u32 spin = 0; spin < SPIN_CAP; spin++) {
                MEMB();
                if (__builtin_amdgcn_readfirstlane(*(LAS u32*)(L + L_FLG)) == tag) break;
                __builtin_amdgcn_s_sleep(2);
            }
            STAMP(5);
        }
        MEMB();
        {
            const u32x4 qf = *(LAS u32x4*)(L + L_Q + (n & 3) * 256 + c * 64 + (n >> 2) * 16);
            const f32x4 z = {0.f, 0.f, 0.f, 0.f};
            float sc[8];
#pragma unroll
            for (int j = 0; j < 8; j++) {
                const f32x4 t = mfma16(rr[j], qf, z);
                float v = (n & 8) ? ((n & 4) ? t[3] : t[2]) : ((n & 4) ? t[1] : t[0]);
                v += dpp<ROR4>(v); v += dpp<ROR8>(v);
                sc[j] = rbf(rbf(v) * 0.08838834764831845f) * LOG2E;
            }
            float mx = fmaxf(fmaxf(fmaxf(sc[0], sc[1]), fmaxf(sc[2], sc[3])), fmaxf(fmaxf(sc[4], sc[5]), fmaxf(sc[6], sc[7])));
            mx = x16_max(mx);
            float p[8];
#pragma unroll
            for (int j = 0; j < 8; j++) p[j] = __builtin_amdgcn_exp2f(sc[j] - mx);
            const float ls = x16_sum(((p[0] + p[1]) + (p[2] + p[3])) + ((p[4] + p[5]) + (p[6] + p[7])));
            if (n < 4) {
                *(LAS f32x4*)(L + L_PP + dw * 512 + (c * 4 + n) * 32) = f32x4{p[0], p[1], p[2], p[3]};
                *(LAS f32x4*)(L + L_PP + dw * 512 + (c * 4 + n) * 32 + 16) = f32x4{p[4], p[5], p[6], p[7]};
                *(LAS float*)(L + L_MX + n * 32 + dw * 4) = mx;
                *(LAS float*)(L + L_LS + n * 32 + dw * 4) = ls;
            }
        }
        STAMP(6);
        if (dw == 0) {
            u32x2 p0, p1;
#pragma unroll 1
            for (u32 spin = 0; spin < SPIN_CAP; spin++) {
                MEMB();
                p0 = *(LAS u32x2*)(L + L_TP + lane * 8);
                p1 = *(LAS u32x2*)(L + L_TP + 512 + lane * 8);
                if (__all(p0[1] == tag && (c == 3 || p1[1] == tag))) break;
            }
            const float y = x16_sum(__uint_as_float(p0[0]) + (c == 3 ? 0.f : __uint_as_float(p1[0])));
            const float yy = rbf(y) + bflo(b2v);
            ST8((u32x2{pack2(yy, dpp<XOR1>(yy)), tag}), (lane & 49) ? OOB : WS_TAB + wg * 128 + lane * 4);
        }
        asm volatile("" :: "v"(pre));
        LGK0();
        STAMP(7);
        FIN();
        asm volatile(".Lc_end:");
    }
}

extern "C" void kernel_launch(void* const* d_in, const int* in_sizes, int n_in,
                              void* d_out, int out_size, void* d_ws, size_t ws_size,
                              hipStream_t stream) {
    (void)in_sizes; (void)n_in; (void)out_size; (void)ws_size;
    (void)hipFuncSetAttribute((const void*)fused_kernel, hipFuncAttributeMaxDynamicSharedMemorySize, LDS_BYTES);
    fused_kernel<<<256, 1024, LDS_BYTES, stream>>>(
        (const u16*)d_in[1], (const u16*)d_in[2], (unsigned char*)d_ws, (const u16*)d_in[3],
        (const u16*)d_in[0], (const u16*)d_in[4],
        (const u16*)d_in[6], (const u16*)d_in[7], (const u16*)d_in[8], (const u16*)d_in[9],
        (u16*)d_out);
}
```

```cpp
#include <hip/hip_runtime.h>
#include <stdint.h>

typedef unsigned short u16;
typedef unsigned int u32;
typedef unsigned long long u64;
typedef __attribute__((ext_vector_type(4))) u32 u32x4;
typedef __attribute__((ext_vector_type(2))) u32 u32x2;
typedef __attribute__((ext_vector_type(4))) float f32x4;
typedef __attribute__((ext_vector_type(2))) float f32x2;
typedef __attribute__((ext_vector_type(8))) short bf16x8;
typedef __attribute__((ext_vector_type(4))) short s16x4;
typedef __attribute__((ext_vector_type(2))) __bf16 bf16x2;
#define LAS __attribute__((address_space(3)))

constexpr u32 TQ = 260;
constexpr u32 TV = 300, DV = 30;
constexpr u32 TWO = 620, DWO = 25;
constexpr u32 TA = 720;
constexpr int KS = 6;

constexpr int D = 512, SK = 8192, DH = 128, W = 2048, F = 1792;
constexpr float EPS = 1e-5f;
constexpr float LOG2E = 1.4426950408889634f;
constexpr u32 SPIN_CAP = 1u << 18;
constexpr u32 TEXP0 = 114;

constexpr int WS_EPOCH = 0;
constexpr int WS_Q   = 4096;
constexpr int WS_LSE = WS_Q + 524288;
constexpr int WS_TAB = WS_LSE + 8192;
constexpr int WS_P   = WS_TAB + 16384;
constexpr int WS_O   = WS_P + 65536;
constexpr int WS_CLR = WS_O + 262144;
constexpr int WS_END = WS_CLR + 32768;
constexpr u32 EMPTY = 0xFFFFFFFFu;
constexpr int OOB = 0x7ffffff0;

constexpr int L_WQ   = 0;
constexpr int L_XN   = 16384;
constexpr int L_X    = L_XN + 4096;
constexpr int L_Q    = L_X + 4096;
constexpr int L_FLG  = L_Q + 1024;
constexpr int L_TP   = L_FLG + 256;
constexpr int L_MX   = L_TP + 512;
constexpr int L_S2   = L_MX + 128;
constexpr int L_PP   = L_S2 + 128;
constexpr int L_R2   = L_PP + 4096;
constexpr int L_PART = L_R2 + 4096;
constexpr int L_WO   = L_PART + 16384 + 2048;
constexpr int L_AT   = L_WO + 16384;
constexpr int L_TAB  = L_AT + 16384;
constexpr int L_PAR  = L_TAB + 8192;
constexpr int L_END  = L_PAR + 3 * F * 2;
constexpr int LDS_BYTES = 108 * 1024;
static_assert(L_END <= LDS_BYTES && L_WO % 4096 == 0 && L_AT % 128 == 0, "lds");

__device__ __forceinline__ float bflo(u32 v) { return __uint_as_float(v << 16); }
__device__ __forceinline__ float bfhi(u32 v) { return __uint_as_float(v & 0xFFFF0000u); }
__device__ __forceinline__ u32 pack2(float lo, float hi) {
    bf16x2 v = {(__bf16)lo, (__bf16)hi};
    return __builtin_bit_cast(u32, v);
}
__device__ __forceinline__ float rbf(float x) { return (float)(__bf16)x; }
template <int CTRL> __device__ __forceinline__ float dpp(float x) {
    return __builtin_bit_cast(float, __builtin_amdgcn_mov_dpp(__builtin_bit_cast(int, x), CTRL, 0xf, 0xf, true));
}
__device__ __forceinline__ float sw32(float a, float b) {
    auto t = __builtin_amdgcn_permlane32_swap(__float_as_uint(a), __float_as_uint(b), false, false);
    return __uint_as_float(t[0]) + __uint_as_float(t[1]);
}
__device__ __forceinline__ float sw16(float a, float b) {
    auto t = __builtin_amdgcn_permlane16_swap(__float_as_uint(a), __float_as_uint(b), false, false);
    return __uint_as_float(t[0]) + __uint_as_float(t[1]);
}
__device__ __forceinline__ float x16_sum(float x) { x = sw16(x, x); return sw32(x, x); }
__device__ __forceinline__ float x16_max(float x) {
    auto s = __builtin_amdgcn_permlane16_swap(__float_as_uint(x), __float_as_uint(x), false, false);
    x = fmaxf(__uint_as_float(s[0]), __uint_as_float(s[1]));
    auto t = __builtin_amdgcn_permlane32_swap(__float_as_uint(x), __float_as_uint(x), false, false);
    return fmaxf(__uint_as_float(t[0]), __uint_as_float(t[1]));
}
constexpr int XOR1 = 0xB1, XOR2 = 0x4E, ROR4 = 0x124, ROR8 = 0x128, HMIR = 0x141;
__device__ __forceinline__ float row_sum(float x) {
    x += dpp<XOR1>(x); x += dpp<XOR2>(x); x += dpp<ROR4>(x); x += dpp<ROR8>(x);
    return x;
}
__device__ __forceinline__ float wave_sum(float x) { return x16_sum(row_sum(x)); }
__device__ __forceinline__ f32x4 mfma16(u32x4 a, u32x4 b, f32x4 c) {
    return __builtin_amdgcn_mfma_f32_16x16x32_bf16(__builtin_bit_cast(bf16x8, a), __builtin_bit_cast(bf16x8, b), c, 0, 0, 0);
}
__device__ __forceinline__ u32x4 trfrag(LAS unsigned char* lo, LAS unsigned char* hi) {
    const u32x2 l = __builtin_bit_cast(u32x2, __builtin_amdgcn_ds_read_tr16_b64_v4i16((LAS s16x4*)lo));
    const u32x2 h = __builtin_bit_cast(u32x2, __builtin_amdgcn_ds_read_tr16_b64_v4i16((LAS s16x4*)hi));
    return u32x4{l[0], l[1], h[0], h[1]};
}
#define SB() __builtin_amdgcn_sched_barrier(0)
#define MEMB() asm volatile("" ::: "memory")
#define BARRIER() do { asm volatile("s_waitcnt lgkmcnt(0)\n\ts_barrier" ::: "memory"); } while (0)
#define LGK0() asm volatile("s_waitcnt lgkmcnt(0)" ::: "memory")
constexpr int SC1 = 16, NT = 2;
#define ST8(v, off) __builtin_amdgcn_raw_buffer_store_b64(v, rs, off, 0, SC1)
#define ST16(v, off) __builtin_amdgcn_raw_buffer_store_b128(v, rs, off, 0, SC1)

#define G_LD(i, so) "s_mov_b64 exec, %[m" #i "]\n\tbuffer_load_dwordx4 %[a" #i "], %[vo], %[rs], " so " offen sc1\n\t"
#define G_END "s_mov_b64 exec, -1\n\ts_waitcnt vmcnt(0)"
template <int N>
__device__ __forceinline__ void poll(u32x4 (&a)[N], __amdgpu_buffer_rsrc_t rs, int off, int stride) {
#pragma unroll
    for (int i = 0; i < N; i++) a[i] = __builtin_amdgcn_raw_buffer_load_b128(rs, off, i * stride, SC1);
    SB();
}
template <int N, int NAP>
__device__ __forceinline__ void settle(u32x4 (&a)[N], __amdgpu_buffer_rsrc_t rs, int off, int stride, u32 tag) {
    u32 bad = 0;
#pragma unroll
    for (int i = 0; i < N; i++) bad |= (a[i][1] ^ tag) | (a[i][3] ^ tag);
    if (__builtin_expect(__ballot(bad != 0) != 0, 0)) {
#pragma unroll 1
        for (u32 spin = 0; spin < SPIN_CAP; spin++) {
            u64 m[N], any = 0;
#pragma unroll
            for (int i = 0; i < N; i++) { m[i] = __ballot(a[i][1] != tag || a[i][3] != tag); any |= m[i]; }
            if (!any) break;
            if (NAP) __builtin_amdgcn_s_sleep(NAP);
            if constexpr (N == 4)
                asm volatile(G_LD(0, "0") G_LD(1, "%[s1]") G_LD(2, "%[s2]") G_LD(3, "%[s3]") G_END
                    : [a0] "+v"(a[0]), [a1] "+v"(a[1]), [a2] "+v"(a[2]), [a3] "+v"(a[3])
                    : [m0] "s"(m[0]), [m1] "s"(m[1]), [m2] "s"(m[2]), [m3] "s"(m[3]), [vo] "v"(off), [rs] "s"(rs),
                      [s1] "s"(stride), [s2] "s"(2 * stride), [s3] "s"(3 * stride) : "memory");
            else if constexpr (N == 2)
                asm volatile(G_LD(0, "0") G_LD(1, "%[s1]") G_END
                    : [a0] "+v"(a[0]), [a1] "+v"(a[1])
                    : [m0] "s"(m[0]), [m1] "s"(m[1]), [vo] "v"(off), [rs] "s"(rs), [s1] "s"(stride) : "memory");
            else
                asm volatile(G_LD(0, "0") G_END : [a0] "+v"(a[0]) : [m0] "s"(m[0]), [vo] "v"(off), [rs] "s"(rs) : "memory");
        }
    }
}

#define P_CONS(i, z0, z1, z2, z3, w) "s_mov_b64 exec, %[m" #i "]\n\ts_waitcnt vmcnt(" #w ")\n\t" \
    "v_cmpx_eq_u32_e32 vcc, %[tag], v" #z1 "\n\tv_cmpx_eq_u32_e32 vcc, %[tag], v" #z3 "\n\t" \
    "v_mov_b32 %[ra" #i "], v" #z0 "\n\tv_mov_b32 %[rb" #i "], v" #z2 "\n\tv_mov_b32 %[o" #i "], 0x7ffffff0\n\t" \
    "s_andn2_b64 %[m" #i "], %[m" #i "], exec\n\t"
#define P_DONE "s_mov_b64 exec, -1\n\ts_or_b64 vcc, %[m0], %[m1]\n\ts_or_b64 vcc, vcc, %[m2]\n\ts_or_b64 vcc, vcc, %[m3]\n\t" \
    "s_cbranch_scc0 9f\n\ts_sleep %[nap]\n\t"
#define P_LD(i, z0, z3, so) "buffer_load_dwordx4 v[" #z0 ":" #z3 "], %[o" #i "], %[rs], " so " offen sc1\n\t"
template <int H, int NAP>
__device__ __forceinline__ void gather4(u32x4 (&X)[4], u32x4 (&Y)[4], u32x2 (&R)[4], __amdgpu_buffer_rsrc_t rs, int off, int stride, u32 tag) {
    int o0 = off, o1 = off, o2 = off, o3 = off;
    u64 m0 = ~0ull, m1 = ~0ull, m2 = ~0ull, m3 = ~0ull;
    u32 cnt = SPIN_CAP, ra0, ra1, ra2, ra3, rb0, rb1, rb2, rb3;
    asm volatile(
        "1:\n\t"
        P_CONS(0, 96, 97, 98, 99, 7) P_CONS(1, 100, 101, 102, 103, 6) P_CONS(2, 104, 105, 106, 107, 5) P_CONS(3, 108, 109, 110, 111, 4)
        P_DONE
        P_LD(0, 96, 99, "0") P_LD(1, 100, 103, "%[s1]") P_LD(2, 104, 107, "%[s2]") P_LD(3, 108, 111, "%[s3]")
        "s_cmp_eq_u32 %[cnt], %[cap]\n\ts_cbranch_scc0 2f\n\ts_sleep %[h]\n\ts_branch 3f\n"
        "2:\n\t"
        P_CONS(0, 112, 113, 114, 115, 7) P_CONS(1, 116, 117, 118, 119, 6) P_CONS(2, 120, 121, 122, 123, 5) P_CONS(3, 124, 125, 126, 127, 4)
        P_DONE
        "3:\n\t"
        P_LD(0, 112, 115, "0") P_LD(1, 116, 119, "%[s1]") P_LD(2, 120, 123, "%[s2]") P_LD(3, 124, 127, "%[s3]")
        "s_sub_u32 %[cnt], %[cnt], 1\n\ts_cbranch_scc0 1b\n"
        "9:"
        : "+{v[96:99]}"(X[0]), "+{v[100:103]}"(X[1]), "+{v[104:107]}"(X[2]), "+{v[108:111]}"(X[3]),
          "+{v[112:115]}"(Y[0]), "+{v[116:119]}"(Y[1]), "+{v[120:123]}"(Y[2]), "+{v[124:127]}"(Y[3]),
          [ra0] "=&v"(ra0), [ra1] "=&v"(ra1), [ra2] "=&v"(ra2), [ra3] "=&v"(ra3),
          [rb0] "=&v"(rb0), [rb1] "=&v"(rb1), [rb2] "=&v"(rb2), [rb3] "=&v"(rb3),
          [o0] "+v"(o0), [o1] "+v"(o1), [o2] "+v"(o2), [o3] "+v"(o3),
          [m0] "+s"(m0), [m1] "+s"(m1), [m2] "+s"(m2), [m3] "+s"(m3), [cnt] "+s"(cnt)
        : [tag] "s"(tag), [rs] "s"(rs), [s1] "s"(stride), [s2] "s"(2 * stride), [s3] "s"(3 * stride),
          [cap] "n"(SPIN_CAP), [h] "n"(H), [nap] "n"(NAP)
        : "vcc", "scc", "memory");
    R[0] = u32x2{ra0, rb0}; R[1] = u32x2{ra1, rb1}; R[2] = u32x2{ra2, rb2}; R[3] = u32x2{ra3, rb3};
}
__device__ __forceinline__ void drain(const u32x4 (&X)[4], const u32x4 (&Y)[4]) {
    asm volatile("" :: "{v[96:99]}"(X[0]), "{v[100:103]}"(X[1]), "{v[104:107]}"(X[2]), "{v[108:111]}"(X[3]),
                 "{v[112:115]}"(Y[0]), "{v[116:119]}"(Y[1]), "{v[120:123]}"(Y[2]), "{v[124:127]}"(Y[3]));
}

#define Q_CHK(i, z0, z1, z2, z3, w) "s_mov_b64 exec, %[m" #i "]\n\ts_waitcnt vmcnt(" #w ")\n\t" \
    "v_max3_u32 %[t], v" #z0 ", v" #z1 ", v" #z2 "\n\tv_max_u32 %[t], %[t], v" #z3 "\n\tv_cmpx_ne_u32_e32 vcc, -1, %[t]\n\t"
#define Q_END(i) "v_mov_b32 %[o" #i "], 0x7ffffff0\n\ts_andn2_b64 %[m" #i "], %[m" #i "], exec\n\t"
#define Q_LDS(i, z0, z1, z2, z3, w) Q_CHK(i, z0, z1, z2, z3, w) "ds_write_b128 %[w" #i "], v[" #z0 ":" #z3 "]\n\t" Q_END(i)
#define Q_REG(i, z0, z1, z2, z3, w) Q_CHK(i, z0, z1, z2, z3, w) \
    "v_mov_b64 %[ra" #i "], v[" #z0 ":" #z1 "]\n\tv_mov_b64 %[rb" #i "], v[" #z2 ":" #z3 "]\n\t" Q_END(i)
#define Q_DONE "s_mov_b64 exec, -1\n\ts_or_b64 vcc, %[m0], %[m1]\n\ts_cbranch_scc0 9f\n\ts_sleep %[nap]\n\t"
#define Q_LDX P_LD(0, 112, 115, "0") P_LD(1, 116, 119, "%[s1]")
#define Q_LDY P_LD(0, 120, 123, "0") P_LD(1, 124, 127, "%[s1]")
#define Q_BODY(C) \
        Q_LDX \
        ".if %[e]\n\ts_sleep %[h]\n\t" Q_LDY ".else\n\ts_waitcnt vmcnt(0)\n\t.endif\n" \
        "1:\n\t" \
        C(0, 112, 113, 114, 115, 3) C(1, 116, 117, 118, 119, 2) \
        Q_DONE Q_LDX \
        ".if !%[e]\n\ts_cmp_eq_u32 %[cnt], %[cap]\n\ts_cbranch_scc0 2f\n\ts_sleep %[h]\n\ts_branch 3f\n\t.endif\n" \
        "2:\n\t" \
        C(0, 120, 121, 122, 123, 3) C(1, 124, 125, 126, 127, 2) \
        Q_DONE \
        "3:\n\t" \
        Q_LDY \
        "s_sub_u32 %[cnt], %[cnt], 1\n\ts_cbranch_scc0 1b\n" \
        "9:"
#define Q_BUFS "+{v[112:115]}"(X[0]), "+{v[116:119]}"(X[1]), "+{v[120:123]}"(X[2]), "+{v[124:127]}"(X[3])
template <int H, int NAP, int E>
__device__ __forceinline__ void gather2_lds(u32x4 (&X)[4], u32 w0, u32 w1, __amdgpu_buffer_rsrc_t rs, int o0, int o1, int stride) {
    u64 m0 = ~0ull, m1 = ~0ull;
    u32 cnt = SPIN_CAP, t;
    asm volatile(Q_BODY(Q_LDS)
        : Q_BUFS, [t] "=&v"(t), [o0] "+v"(o0), [o1] "+v"(o1), [m0] "+s"(m0), [m1] "+s"(m1), [cnt] "+s"(cnt)
        : [w0] "v"(w0), [w1] "v"(w1), [rs] "s"(rs), [s1] "s"(stride), [cap] "n"(SPIN_CAP), [h] "n"(H), [nap] "n"(NAP), [e] "n"(E)
        : "vcc", "scc", "memory");
}
template <int H, int NAP, int E>
__device__ __forceinline__ void gather2_reg(u32x4 (&X)[4], u32x2 (&R)[4], __amdgpu_buffer_rsrc_t rs, int o0, int o1, int stride) {
    u64 m0 = ~0ull, m1 = ~0ull;
    u32 cnt = SPIN_CAP, t;
    asm volatile(Q_BODY(Q_REG)
        : Q_BUFS, [t] "=&v"(t), [ra0] "=&v"(R[0]), [rb0] "=&v"(R[1]), [ra1] "=&v"(R[2]), [rb1] "=&v"(R[3]),
          [o0] "+v"(o0), [o1] "+v"(o1), [m0] "+s"(m0), [m1] "+s"(m1), [cnt] "+s"(cnt)
        : [rs] "s"(rs), [s1] "s"(stride), [cap] "n"(SPIN_CAP), [h] "n"(H), [nap] "n"(NAP), [e] "n"(E)
        : "vcc", "scc", "memory");
}
__device__ __forceinline__ void drain2(const u32x4 (&X)[4]) {
    asm volatile("" :: "{v[112:115]}"(X[0]), "{v[116:119]}"(X[1]), "{v[120:123]}"(X[2]), "{v[124:127]}"(X[3]));
}

constexpr float GK0 = -2.0f * 0.7978845608028654f * LOG2E, GK1 = GK0 * 0.044715f;
__device__ __forceinline__ f32x2 unit2(float t, f32x2 w1, f32x2 b1, f32x2 w2, f32x2 acc) {
    const f32x2 u = __builtin_elementwise_fma(f32x2{t, t}, w1, b1);
    const f32x2 z = u * __builtin_elementwise_fma(u * u, f32x2{GK1, GK1}, f32x2{GK0, GK0});
    const f32x2 e = f32x2{__builtin_amdgcn_exp2f(z[0]), __builtin_amdgcn_exp2f(z[1])} + f32x2{1.f, 1.f};
    const f32x2 r = {__builtin_amdgcn_rcpf(e[0]), __builtin_amdgcn_rcpf(e[1])};
    return __builtin_elementwise_fma(u * w2, r, acc);
}
__device__ __forceinline__ f32x2 un2(u32 v) { return f32x2{bflo(v), bfhi(v)}; }

#define RSRC(p, nbytes) __builtin_amdgcn_make_buffer_rsrc((void*)(p), 0, nbytes, 0x00020000)
#define LDNT(r, v, s) __builtin_amdgcn_raw_buffer_load_b128(r, v, s, NT)
#define WOF ((8 * ((lane >> 2) & 3) + 2 * (lane >> 5) + ((lane >> 4) & 1)) * 1024 + ((lane & 3) ^ (2 * (lane >> 5))) * 16)
template <int K> __device__ __forceinline__ float qbc(float x) { return dpp<K * 0x55>(x); }

extern "C" __global__ void __launch_bounds__(1024, 4)
fused_kernel(const u16* __restrict__ kc, const u16* __restrict__ vc, unsigned char* ws, const u16* __restrict__ wq,
             const u16* __restrict__ x, const u16* __restrict__ wo,
             const u16* __restrict__ w1, const u16* __restrict__ b1, const u16* __restrict__ w2,
             const u16* __restrict__ b2, u16* out) {
    extern __shared__ __attribute__((aligned(16))) unsigned char smem[];
    LAS unsigned char* L = (LAS unsigned char*)smem;
    const int wg = blockIdx.x, kvh = wg & 7, r = wg >> 3, b = r >> 3, s = r & 7;
    const int lane = threadIdx.x & 63;
    const int wave = __builtin_amdgcn_readfirstlane(threadIdx.x >> 6);
    const int c = lane >> 4, n = lane & 15, dw = wave & 7;
    const bool cons = (wg & 0xF9) == 0;
    const int bj = wg >> 1;
    const int rho = r & 1, cg = r >> 1;
    const u32 kvoff = ((u32)(b * 8 + kvh) * SK + (SK - W) + s * 256) * (DH * 2);

    const u32 zm = (s << 2) | b | ((kvh & 1) << 5);
    const u32 kz = (((zm >> 2) & 3) ^ (wave & 2)) * 4096;
    u32x4 rr[16];
    u32 b2v, ep, pre;
    u64 ts64, pc;
    u32 len, tmp;
    int ao, ao1, ao2, ao3;
    __amdgpu_buffer_rsrc_t ra;
    {
        const u32 am = (u32)((wave - 8) >> 31);
        u32 aoff = (((b * 128 + dw * 16) * 8192 + kvh * 1024 + s * 128) & am) | ((kvoff + ((dw & 3) ^ (zm >> 4)) * 16384) & ~am), asz = ((15 * 8192 + 128) & am) | (16384 & ~am);
        ao = (((((lane >> 3) & 3) + (lane >> 5) * 8) * 8192 + ((lane & 7) ^ (c * 2)) * 16) & am) | (((n >> 2) * 256 + c * 64 + (n & 3) * 16 + (zm & 3) * 1024) & ~am);
        int po = (((dw & 3) * 1024 + lane * 16) & am) | (((dw & 3) * 896 + min(lane, 55) * 16) & ~am);
        asm volatile("s_getpc_b64 %0\n.Lpc_here:\n\ts_mov_b32 %1, .Lc_end-.Lpc_here+192" : "=s"(pc), "=s"(len));
        pc += min((u32)(r * 8 + dw) * 64, len) & ~am;
        asm volatile("s_setprio 2" : "+s"(aoff), "+s"(asz), "+v"(ao), "+v"(po), "+s"(pc));
        asm volatile("" :: "s"(kc), "s"(vc), "s"(ws), "s"(wq), "s"(x), "s"(wo), "s"(w1), "s"(b1), "s"(w2), "s"(b2), "s"(out));
        ep = *(const __attribute__((address_space(4))) u32*)(ws + WS_EPOCH);
        pre = *(const __attribute__((address_space(4))) u32*)pc;
        ts64 = __builtin_amdgcn_s_memrealtime();
        const u64 am2 = (u64)(long long)(int)am;
        ra = RSRC((((u64)wq & am2) | ((u64)kc & ~am2)) + aoff, asz);
#define KL(i, so, io) "buffer_load_dwordx4 %[r" #i "], %[ao" #io "], %[ra], " so " offen nt\n\t"
#define KC(op, i, so, io) ".if " #i " " #op " %[ks]\n\t" KL(i, so, io) ".endif\n\t"
#define KALL(op) \
            KC(op, 4, "%[s1]", 0) KC(op, 5, "%[s1]", 1) KC(op, 6, "%[s1]", 2) KC(op, 7, "%[s1]", 3) \
            KC(op, 8, "%[s2]", 0) KC(op, 9, "%[s2]", 1) KC(op, 10, "%[s2]", 2) KC(op, 11, "%[s2]", 3) \
            KC(op, 12, "%[s3]", 0) KC(op, 13, "%[s3]", 1) KC(op, 14, "%[s3]", 2) KC(op, 15, "%[s3]", 3)
        asm volatile(
            "s_cmp_lt_u32 %[wv], 8\n\ts_cbranch_scc0 1f\n\t"
            "global_load_dwordx4 %[r0], %[po], %[x]\n\t"
            KL(1, "0", 0) KL(2, "%[s0]", 0)
            "s_barrier\n\t"
            "s_branch 2f\n"
            "1:\n\t"
            "s_cmp_lt_u32 %[wv], 12\n\ts_cbranch_scc0 3f\n\t"
            "s_cmp_eq_u32 %[wv], 8\n\ts_cbranch_scc0 4f\n\t"
            "s_lshl_b32 %[t], %[kvh], 5\n\ts_add_u32 %[t], %[t], %[r]\n\ts_lshl_b32 %[t], %[t], 10\n\ts_add_u32 %[t], %[t], %[wso]\n\t"
            "v_lshl_add_u32 v100, %[tid], 4, %[t]\n\t"
            "v_mov_b32 v96, -1\n\tv_mov_b32 v97, -1\n\tv_mov_b32 v98, -1\n\tv_mov_b32 v99, -1\n\t"
            "global_store_dwordx4 v100, v[96:99], %[ws] sc1\n\t"
            "4:\n\t"
            "global_load_dwordx4 %[r0], %[po], %[w1]\n\t"
            "global_load_dwordx4 %[r1], %[po], %[b1]\n\t"
            "global_load_dwordx4 %[r2], %[po], %[w2]\n\t"
            "global_load_ushort %[p3], %[z], %[b2]\n\t"
            "s_barrier\n\t"
            "s_setprio 0\n\t"
            "s_waitcnt vmcnt(0)\n\t"
            "s_branch 2f\n"
            "3:\n\t"
            "v_xor_b32 %[ao1], 0x400, %[ao0]\n\tv_xor_b32 %[ao2], 0x800, %[ao0]\n\tv_xor_b32 %[ao3], 0xc00, %[ao0]\n\t"
            "s_barrier\n\t"
            "s_setprio 1\n\t"
            KL(0, "%[sz]", 0)
            KL(1, "%[sz]", 1)
            KL(2, "%[sz]", 2)
            KL(3, "%[sz]", 3)
            KALL(<)
            "2:\n\t"
            "s_setprio 0"
            : [r0] "=&v"(rr[0]), [r1] "=&v"(rr[1]), [r2] "=&v"(rr[2]), [r3] "=&v"(rr[3]), [r4] "=&v"(rr[4]), [r5] "=&v"(rr[5]), [r6] "=&v"(rr[6]), [r7] "=&v"(rr[7]),
              [r8] "=&v"(rr[8]), [r9] "=&v"(rr[9]), [r10] "=&v"(rr[10]), [r11] "=&v"(rr[11]), [r12] "=&v"(rr[12]), [r13] "=&v"(rr[13]), [r14] "=&v"(rr[14]), [r15] "=&v"(rr[15]),
              [p3] "=&v"(b2v), [t] "=&s"(tmp), [ao1] "=&v"(ao1), [ao2] "=&v"(ao2), [ao3] "=&v"(ao3)
            : [wv] "s"(wave), [po] "v"(po), [ao0] "v"(ao), [ra] "s"(ra), [x] "s"(x), [w1] "s"(w1), [b1] "s"(b1), [w2] "s"(w2), [b2] "s"(b2),
              [z] "v"(0), [s0] "s"(4 * 8192), [sz] "s"(kz), [s1] "s"(kz ^ 4096), [s2] "s"(kz ^ 8192), [s3] "s"(kz ^ 12288),
              [kvh] "s"(kvh), [r] "s"(r), [wso] "n"(WS_O - 8192), [tid] "v"(threadIdx.x), [ws] "s"(ws), [ks] "n"(KS)
            : "scc", "vcc", "memory", "v96", "v97", "v98", "v99", "v100", "v127");
    }
    asm volatile("" : "+s"(ep), "+s"(pre), "+s"(ts64));
    const u32 tstart = (u32)ts64;
    const __amdgpu_buffer_rsrc_t rs = RSRC(ws, WS_END);
    const u32 tag = (ep + 0x9E3779B9u) | 1u;
    const int pslot = WS_P + (cg >> 1) * 2048 + (kvh * 2 + rho) * 128 + (cg & 1) * 64;

    if (__builtin_expect(wave < 8, 1)) {
        if (dw < 4) {
            asm volatile("s_waitcnt vmcnt(2)" : "+v"(rr[0]));
            const u32x4 xr = rr[0];
            float v[8];
#pragma unroll
            for (int k = 0; k < 4; k++) { v[2 * k] = bflo(xr[k]); v[2 * k + 1] = bfhi(xr[k]); }
            const float mean = wave_sum(((v[0] + v[1]) + (v[2] + v[3])) + ((v[4] + v[5]) + (v[6] + v[7]))) * (1.0f / D);
            float ss = 0.f;
#pragma unroll
            for (int k = 0; k < 8; k++) { const float d = v[k] - mean; ss = fmaf(d, d, ss); }
            const float var = rbf(wave_sum(ss) * (1.0f / D));
            const float m = rbf(mean);
            const float inv = __builtin_amdgcn_rcpf(rbf(__builtin_amdgcn_sqrtf(rbf(var + EPS))));
            u32x4 o;
#pragma unroll
            for (int k = 0; k < 4; k++) o[k] = pack2(rbf(v[2 * k] - m) * inv, rbf(v[2 * k + 1] - m) * inv);
            *(LAS u32x4*)(L + L_XN + dw * 1024 + lane * 16) = o;
            *(LAS u32x4*)(L + L_X + dw * 1024 + lane * 16) = xr;
        }
        {
            LAS unsigned char* wb = L + L_WQ + dw * 2048 + (lane >> 5) * 1024 + (lane & 31) * 16;
            asm volatile("s_waitcnt vmcnt(0)" : "+v"(rr[0]), "+v"(rr[1]), "+v"(rr[2]));
            *(LAS u32x4*)(wb) = rr[1];
            *(LAS u32x4*)(wb + 512) = rr[2];
            LGK0();
            *(LAS u32*)(L + L_FLG + 96 + dw * 4) = tag;
        }
        if (dw >= 4) {
            const int cb = dw & 3;
            __builtin_amdgcn_s_setprio(3);
#pragma unroll 1
            for (u32 spin = 0; spin < SPIN_CAP; spin++) {
                MEMB();
                const u32 f = *(LAS u32*)(L + L_FLG + 96 + (lane & 7) * 4);
                if (__ballot(f != tag) == 0) break;
            }
            MEMB();
            LAS unsigned char* tb = L + L_WQ + (8 * c + (n >> 2)) * 128 + (cb ^ (n >> 3) ^ ((c & 1) * 2)) * 32 + (n & 3) * 8;
            LAS unsigned char* ab = L + L_XN + (n & 3) * 1024 + b * 256 + c * 16;
            const f32x4 z = {0.f, 0.f, 0.f, 0.f};
            f32x4 qa = mfma16(*(LAS u32x4*)(ab), trfrag(tb, tb + 512), z);
            f32x4 qb = mfma16(*(LAS u32x4*)(ab + 64), trfrag(tb + 4096, tb + 4608), z);
            qa = mfma16(*(LAS u32x4*)(ab + 128), trfrag(tb + 8192, tb + 8704), qa);
            qb = mfma16(*(LAS u32x4*)(ab + 192), trfrag(tb + 12288, tb + 12800), qb);
            qa += qb;
            const float qv = (c & 2) ? ((c & 1) ? qa[3] : qa[2]) : ((c & 1) ? qa[1] : qa[0]);
            __builtin_amdgcn_raw_buffer_store_b128((u32x4{__float_as_uint(qv), tag, __float_as_uint(dpp<XOR1>(qv)), tag}), rs,
                 (n & 1) ? OOB : WS_Q + (kvh * 4 + c) * 16384 + (s >> 1) * 4096 + b * 1024 + (s & 1) * 512 + cb * 128 + n * 8, 0, SC1);
            SB();
            *(LAS u32*)(L + L_FLG + 16 + cb * 4) = tag;
            __builtin_amdgcn_s_setprio(0);
        }
        SB();
        u32x4 vr[8];
        u32 tba = L_WO + dw * 2048 + (8 * c + (n >> 2)) * 64 + (n & 3) * 8 + (c & 1) * 32;
        int lso = WS_LSE + ((kvh * 4 + rho * 2 + (dw >> 2)) * 32 + (dw & 1) * 16 + (c >> 1) * 8 + ((n & 3) ^ ((c & 1) * 2)) * 2) * 8;
        int clo = WS_CLR + (kvh * 32 + (dw & 1) * 16 + n) * 128;
        int aof = WS_O + (kvh * 32 + (dw & 1) * 16 + (lane >> 3)) * 1024 + (rho * 4 + (dw >> 1)) * 128 + (lane & 7) * 16, aof1 = aof;
        u32 wa0 = L_AT + dw * 2048 + (lane >> 3) * 128 + ((lane & 7) ^ (lane >> 4)) * 16, wa1 = (wa0 ^ 64) + 1024;
        u32 aa = L_AT + dw * 2048 + n * 128 + (((dw & 1) * 4 + c) ^ (n >> 1)) * 16, aa1 = aa ^ 64;
        u32 r2a = L_R2 + dw * 512 + lane * 8, r2b = L_R2 + c * 256 + n * 8;
        int pso = (n & 3) ? OOB : pslot + c * 16384 + n * 4;
        u32 ppa = L_PP + dw * 256 + c * 64 + (n & 3) * 16;
        u32 pba = L_PART + dw * 2048 + c * 256 + (n & 12) * 16 + ((n + 2 * c) & 3) * 16, pbb = L_PART + dw * 2048 + c * 256 + (n & 12) * 16 + 1024 + ((n + 2 * c + 1) & 3) * 16;
        const int col = dw * 64 + ((lane >> 2) & 1) * 32 + (lane >> 5) * 16 + (lane & 3) * 4 + ((lane >> 3) & 3);
        int pof = WS_P + bj * 16384 + dw * 2048 + lane * 16, pof1 = pof, tof = (cons ? WS_TAB + dw * 2048 : WS_END) + lane * 16, oof = (bj * D + col) * 2;
        asm volatile("" : "+v"(lso), "+v"(clo), "+v"(aof), "+v"(aof1), "+v"(wa0), "+v"(wa1), "+v"(aa), "+v"(aa1), "+v"(r2a), "+v"(r2b), "+v"(pso),
                          "+v"(pof), "+v"(pof1), "+v"(tof), "+v"(oof), "+v"(tba), "+v"(ppa), "+v"(pba), "+v"(pbb));
        {
            const __amdgpu_buffer_rsrc_t rv = RSRC((const unsigned char*)vc + kvoff + (dw ^ (zm >> 3)) * 8192, 8192);
            int vo[4];
#pragma unroll
            for (int j = 0; j < 4; j++) vo[j] = (lane * 16 + (zm & 3) * 1024) ^ (j * 1024);
            asm volatile("" : "+v"(vo[0]), "+v"(vo[1]), "+v"(vo[2]), "+v"(vo[3]));
#pragma unroll 1
            for (u32 spin = 0; spin < SPIN_CAP; spin++) {
                MEMB();
                if (__ballot(*(LAS u32*)(L + L_FLG + 80 + (lane & 3) * 4) != tag) == 0 &&
                    (u32)__builtin_amdgcn_s_memrealtime() - tstart >= TV + dw * DV) break;
                __builtin_amdgcn_s_sleep(1);
            }
#pragma unroll
            for (int j = 0; j < 8; j++) { vr[j] = LDNT(rv, vo[j & 3], ((j >> 2) * 4096) ^ ((zm & 4) * 1024)); SB(); }
            *(LAS u32*)(L + L_FLG + 128 + dw * 4) = tag;
        }
#pragma unroll 1
        for (u32 spin = 0; spin < SPIN_CAP; spin++) {
            MEMB();
            if (__builtin_amdgcn_readfirstlane(*(LAS u32*)(L + L_FLG + 48 + (dw >> 1) * 4)) == tag) break;
            __builtin_amdgcn_s_sleep(1);
        }
        MEMB();
        __builtin_amdgcn_s_setprio(2);
        u32x4 g[1], tg[2], a[4];
        u32x4 pfm[4];
        {
            const u32x4 pf = *(LAS u32x4*)(L + ppa);
            const u32x4 zz = {0u, 0u, 0u, 0u};
#pragma unroll
            for (int g = 0; g < 4; g++) pfm[g] = (n >> 2) == g ? pf : zz;
        }
        SB();
        u32x4 cf[8];
#pragma unroll
        for (int w = 0; w < 4; w++) {
#pragma unroll
            for (int d = 0; d < 8; d++) { const int e = (d >> 1) | ((d & 1) << 2); cf[e][w] = __builtin_amdgcn_perm(vr[2 * w + 1][e >> 1], vr[2 * w][e >> 1], (e & 1) ? 0x07060302u : 0x05040100u); }
            if (w < 3) SB();
        }
        {
            const f32x4 z = {0.f, 0.f, 0.f, 0.f};
            f32x4 a0 = mfma16(cf[0], pfm[0], z), a1 = mfma16(cf[4], pfm[0], z);
            a0 = mfma16(cf[1], pfm[1], a0); a1 = mfma16(cf[5], pfm[1], a1);
            a0 = mfma16(cf[2], pfm[2], a0); a1 = mfma16(cf[6], pfm[2], a1);
            a0 = mfma16(cf[3], pfm[3], a0); a1 = mfma16(cf[7], pfm[3], a1);
            *(LAS f32x4*)(L + pba) = a0;
            *(LAS f32x4*)(L + pbb) = a1;
            MEMB();
            *(LAS u32*)(L + L_FLG + 160 + dw * 4) = tag;
            SB();
        }
        __builtin_amdgcn_s_setprio(0);
        u32 cw = __builtin_amdgcn_raw_buffer_load_b32(rs, clo, 0, SC1);
        SB();
        poll<2>(tg, rs, tof, 1024);
        asm volatile("" : "={v[112:115]}"(a[0]), "={v[116:119]}"(a[1]), "={v[120:123]}"(a[2]), "={v[124:127]}"(a[3]));
        float xv = bflo(*(LAS u16*)(L + L_X + (oof & 4095)));
        BARRIER();
        __builtin_amdgcn_s_setprio(2);
        if (dw == 1 && lane < 48) *(LAS u32x4*)(L + L_FLG + lane * 16) = u32x4{0u, 0u, 0u, 0u};
        u32x4 bf0 = trfrag(L + tba, L + tba + 256), bf1 = trfrag(L + (tba ^ 32), L + (tba ^ 32) + 256);
        u32x4 bf2 = trfrag(L + (tba ^ 2048), L + (tba ^ 2048) + 256), bf3 = trfrag(L + (tba ^ 2080), L + (tba ^ 2080) + 256);
        if (__builtin_expect(__ballot(cw != tag) != 0, 0)) {
#pragma unroll 1
            for (u32 spin = 0; spin < SPIN_CAP; spin++) {
                __builtin_amdgcn_s_sleep(4);
                MEMB();
                if (cw != tag) cw = __builtin_amdgcn_raw_buffer_load_b32(rs, clo, 0, SC1);
                if (__ballot(cw != tag) == 0) break;
            }
        }
        MEMB();
        const u32 ttag = cons ? tag : 0u;
        u64 tm0 = __ballot(((tg[0][1] ^ ttag) | (tg[0][3] ^ ttag)) != 0), tm1 = __ballot(((tg[1][1] ^ ttag) | (tg[1][3] ^ ttag)) != 0);
        u64 gm = ~0ull;
        asm volatile("" : "=v"(g[0]));
        asm volatile("" : "+s"(gm), "+s"(tm0), "+s"(tm1), "+v"(bf0), "+v"(bf1), "+v"(bf2), "+v"(bf3), "+v"(tg[0]), "+v"(tg[1]), "+v"(xv));
        {
#pragma unroll 1
            for (u32 spin = 0; spin < 256; spin++) {
                if ((u32)__builtin_amdgcn_s_memrealtime() - tstart >= TA) break;
                __builtin_amdgcn_s_sleep(1);
            }
        }
        {
            asm volatile(G_LD(0, "0") "s_mov_b64 exec, -1" : [a0] "+v"(g[0]) : [m0] "s"(gm), [vo] "v"(lso), [rs] "s"(rs) : "memory");
            asm volatile("s_or_b64 vcc, %[m0], %[m1]\n\ts_cbranch_scc0 1f\n\t" G_LD(0, "0") G_LD(1, "%[s1]") "s_mov_b64 exec, -1\n1:" : [a0] "+v"(tg[0]), [a1] "+v"(tg[1])
                         : [m0] "s"(tm0), [m1] "s"(tm1), [vo] "v"(tof), [rs] "s"(rs), [s1] "s"(1024) : "vcc", "scc", "memory");
            gather2_lds<4, 1, 1>(a, wa0, wa1, rs, aof, aof1, 8 * 1024);
            asm volatile("" : "+v"(g[0]), "+v"(tg[0]), "+v"(tg[1]));
            u32x4 af0 = *(LAS u32x4*)(L + aa);
            u32x4 af1 = *(LAS u32x4*)(L + aa1);
            SB();
            settle<1, 2>(g, rs, lso, 0, tag);
            const float l0 = __uint_as_float(g[0][0]), l1 = __uint_as_float(g[0][2]);
            float M = fmaxf(l0, l1);
            M = fmaxf(M, dpp<XOR1>(M)); M = fmaxf(M, dpp<XOR2>(M));
            const float x0 = __builtin_amdgcn_exp2f(l0 - M), x1 = __builtin_amdgcn_exp2f(l1 - M);
            SB();
            const f32x4 z = {0.f, 0.f, 0.f, 0.f};
            f32x4 o0 = mfma16(af0, bf0, z), o1 = mfma16(af0, bf1, z);
            o0 = mfma16(af1, bf2, o0); o1 = mfma16(af1, bf3, o1);
            float S = x0 + x1;
            S += dpp<XOR1>(S); S += dpp<XOR2>(S);
            const float inv = __builtin_amdgcn_rcpf(S);
            const f32x2 q0 = f32x2{o0[0], o1[0]} * qbc<0>(x0) + f32x2{o0[1], o1[1]} * qbc<0>(x1), q1 = f32x2{o0[2], o1[2]} * qbc<1>(x0) + f32x2{o0[3], o1[3]} * qbc<1>(x1);
            *(LAS f32x2*)(L + r2a) = (q0 + q1) * inv;
        }
        BARRIER();
        if (dw == 0) {
            LAS unsigned char* pb = L + r2b;
            f32x2 q0 = *(LAS f32x2*)(pb) + *(LAS f32x2*)(pb + 128), q1 = *(LAS f32x2*)(pb + 1024) + *(LAS f32x2*)(pb + 1152);
            f32x2 q2 = *(LAS f32x2*)(pb + 2048) + *(LAS f32x2*)(pb + 2176), q3 = *(LAS f32x2*)(pb + 3072) + *(LAS f32x2*)(pb + 3200);
            q0 = (q0 + q1) + (q2 + q3);
            const float pk = __uint_as_float(min(pack2(q0[0], q0[1]), EMPTY - 1));
            ST16((u32x4{__float_as_uint(pk), __float_as_uint(qbc<1>(pk)), __float_as_uint(qbc<2>(pk)), __float_as_uint(qbc<3>(pk))}), pso);
        }
        if (cons) {
            settle<2, 2>(tg, rs, tof, 1024, tag);
            *(LAS u32x2*)(L + L_TAB + dw * 1024 + lane * 8) = u32x2{tg[0][0], tg[0][2]};
            *(LAS u32x2*)(L + L_TAB + dw * 1024 + 512 + lane * 8) = u32x2{tg[1][0], tg[1][2]};
            __builtin_amdgcn_s_sleep(8);
            u32x2 pv[4];
            gather2_reg<4, 0, 0>(a, pv, rs, pof, pof1, 1024);
            const float v0 = sw32(bflo(pv[0][0]) + bflo(pv[2][0]), bfhi(pv[0][0]) + bfhi(pv[2][0]));
            const float v1 = sw32(bflo(pv[0][1]) + bflo(pv[2][1]), bfhi(pv[0][1]) + bfhi(pv[2][1]));
            const float v2 = sw32(bflo(pv[1][0]) + bflo(pv[3][0]), bfhi(pv[1][0]) + bfhi(pv[3][0]));
            const float v3 = sw32(bflo(pv[1][1]) + bflo(pv[3][1]), bfhi(pv[1][1]) + bfhi(pv[3][1]));
            const float u0 = sw16(v0, v2), u1 = sw16(v1, v3);
            const float at = ((lane & 8) ? u1 : u0) + dpp<ROR8>((lane & 8) ? u0 : u1);
            const float h = rbf(xv + rbf(at));
            const float piv = __uint_as_float(__builtin_amdgcn_readfirstlane(__float_as_uint(h)));
            const float d = h - piv;
            const float s1 = wave_sum(d), s2 = wave_sum(d * d);
            if (lane == 0) *(LAS f32x2*)(L + L_S2 + dw * 8) = f32x2{fmaf(s1, 1.0f / 64, piv), fmaf(-s1 * (1.0f / 64), s1, s2)};
            BARRIER();
            const f32x2 ms = *(LAS f32x2*)(L + L_S2 + (lane & 7) * 8);
            float mean = ms[0];
            mean += dpp<XOR1>(mean); mean += dpp<XOR2>(mean); mean += dpp<HMIR>(mean);
            mean *= 0.125f;
            const float dv = ms[0] - mean;
            float m2 = fmaf(dv * 64.f, dv, ms[1]);
            m2 += dpp<XOR1>(m2); m2 += dpp<XOR2>(m2); m2 += dpp<HMIR>(m2);
            const float var = fmaxf(m2 * (1.0f / D), 0.f);
            const float m1 = rbf(mean);
            const float inv1 = __builtin_amdgcn_rcpf(rbf(__builtin_amdgcn_sqrtf(rbf(rbf(var) + EPS))));
            const float t1 = rbf(rbf(h - m1) * inv1);
            const float mb = rbf((mean - m1) * inv1);
            const float inv2 = __builtin_amdgcn_rcpf(rbf(__builtin_amdgcn_sqrtf(rbf(rbf(var * inv1 * inv1) + EPS))));
            const u32 tb = pack2(rbf(t1 - mb) * inv2, 0.f) & 0xFFFFu;
            const u32 ti = (tb & 0x7FFFu) - (TEXP0 << 7);
            float y = bflo(*(LAS u16*)(L + L_TAB + ((ti & 2047u) + (tb >> 15) * 2048u) * 2));
            bool miss = ti >= 2048u;
            u64 mm = __ballot(miss);
            while (__builtin_expect(mm != 0, 0)) {
                const u32 tv = __builtin_amdgcn_readlane(tb, __builtin_ctzll(mm));
                const float t = bflo(tv);
                MEMB();
                u32x4 pa[3][3];
                u32x2 pc2[3];
#pragma unroll
                for (int k = 0; k < 3; k++) {
#pragma unroll
                    for (int it = 0; it < 3; it++) pa[it][k] = *(LAS u32x4*)(L + L_PAR + k * F * 2 + it * 1024 + lane * 16);
                    pc2[k] = *(LAS u32x2*)(L + L_PAR + k * F * 2 + 3072 + lane * 8);
                }
                f32x2 ac0 = unit2(t, un2(pc2[0][0]), un2(pc2[1][0]), un2(pc2[2][0]), f32x2{0.f, 0.f});
                f32x2 ac1 = unit2(t, un2(pc2[0][1]), un2(pc2[1][1]), un2(pc2[2][1]), f32x2{0.f, 0.f});
#pragma unroll
                for (int it = 0; it < 3; it++)
#pragma unroll
                    for (int k = 0; k < 4; k += 2) {
                        ac0 = unit2(t, un2(pa[it][0][k]), un2(pa[it][1][k]), un2(pa[it][2][k]), ac0);
                        ac1 = unit2(t, un2(pa[it][0][k + 1]), un2(pa[it][1][k + 1]), un2(pa[it][2][k + 1]), ac1);
                    }
                ac0 += ac1;
                const float yy = rbf(rbf(wave_sum(ac0[0] + ac0[1])) + bflo(*(LAS u32*)(L + L_S2 + 64)));
                if (tb == tv) { y = yy; miss = false; }
                asm volatile(".Lc_end:");
                mm = __ballot(miss);
            }
            *(u16*)((unsigned char*)out + (u32)oof) = (u16)pack2(h + y, 0.f);
            if (wg == 0 && dw == 0 && lane == 0) __builtin_amdgcn_raw_buffer_store_b32(tag, rs, WS_EPOCH, 0, SC1);
        }
        SB(); drain2(a);
        asm volatile("s_endpgm" : "=v"(rr[0]), "=v"(rr[1]), "=v"(rr[2]), "=v"(rr[3]), "=v"(rr[4]), "=v"(rr[5]), "=v"(rr[6]), "=v"(rr[7]),
                     "=v"(rr[8]), "=v"(rr[9]), "=v"(rr[10]), "=v"(rr[11]), "=v"(rr[12]), "=v"(rr[13]), "=v"(rr[14]), "=v"(rr[15]), "=v"(b2v), "=v"(ao), "=v"(ao1), "=v"(ao2), "=v"(ao3) :: "memory");
        __builtin_amdgcn_s_waitcnt(0);
    }
    {
        if (wave < 12) {
            const int tw = dw & 3;
            if (__builtin_expect(tw == 0, 1)) {
                const u32 cv = lane < 16 ? EMPTY : tag;
                *(LAS u32*)(L + L_FLG + 192) = tag;
                __builtin_amdgcn_s_setprio(3);
                ST16((u32x4{cv, cv, cv, cv}), lane < 16 ? pslot + (lane >> 2) * 16384 + (lane & 3) * 16 : lane == 16 ? WS_CLR + (kvh * 32 + r) * 128 : OOB);
                SB();
                __builtin_amdgcn_s_setprio(0);
            }
            if (cons && lane < 56) {
                *(LAS u32x4*)(L + L_PAR + tw * 896 + lane * 16) = rr[0];
                *(LAS u32x4*)(L + L_PAR + F * 2 + tw * 896 + lane * 16) = rr[1];
                *(LAS u32x4*)(L + L_PAR + F * 4 + tw * 896 + lane * 16) = rr[2];
                *(LAS u32*)(L + L_S2 + 64) = b2v;
            }
            f32x2 pw1[4], pb1[4], pw2[4];
#pragma unroll
            for (int k = 0; k < 4; k++) { pw1[k] = un2(rr[0][k]); pb1[k] = un2(rr[1][k]); pw2[k] = un2(lane < 56 ? rr[2][k] : 0u); }
            const u32 seg = (cg >> 1) * 32 + kvh * 4 + (r & 3);
            const u32 t0 = ((seg & 128) << 24) + (((TEXP0 << 7) + (seg & 127) * 16) << 16);
#pragma unroll 1
            for (int it = 0; it < 4; it++) {
                float a[4];
#pragma unroll
                for (int k = 0; k < 4; k++) {
                    const float t = __uint_as_float(t0 + ((u32)(it * 4 + k) << 16));
                    f32x2 q = {0.f, 0.f};
#pragma unroll
                    for (int i = 0; i < 4; i++) q = unit2(t, pw1[i], pb1[i], pw2[i], q);
                    a[k] = q[0] + q[1];
                }
                const float v = row_sum(sw16(sw32(a[0], a[2]), sw32(a[1], a[3])));
                if (n == 0) *(LAS u32x2*)(L + L_TP + tw * 128 + (it * 4 + c) * 8) = u32x2{__float_as_uint(v), tag};
            }
            if (tw == 0) {
                u32x2 p0;
#pragma unroll 1
                for (u32 spin = 0; spin < SPIN_CAP; spin++) {
                    MEMB();
                    p0 = *(LAS u32x2*)(L + L_TP + lane * 8);
                    if (__all(p0[1] == tag)) break;
                }
                asm volatile("s_waitcnt vmcnt(0)" ::: "memory");
                __builtin_amdgcn_s_setprio(3);
                const float yy = rbf(x16_sum(__uint_as_float(p0[0]))) + bflo(b2v);
                const u32 y2 = pack2(yy, dpp<XOR1>(yy));
                ST16((u32x4{y2, tag, (u32)__builtin_amdgcn_mov_dpp((int)y2, XOR2, 0xf, 0xf, true), tag}), (lane & 51) ? OOB : WS_TAB + seg * 64 + lane * 4);
            }
            {
                const __amdgpu_buffer_rsrc_t rw = RSRC(wo + (size_t)(kvh * 512 + rho * 256 + tw * 64) * D + cg * 32, 63 * 1024 + 64);
                const int wof = WOF;
                __builtin_amdgcn_s_setprio(1);
#pragma unroll 1
                for (u32 spin = 0; spin < SPIN_CAP; spin++) {
                    MEMB();
                    if (__ballot(*(LAS u32*)(L + L_FLG + 128 + (lane & 7) * 4) != tag) == 0 && (u32)__builtin_amdgcn_s_memrealtime() - tstart >= TWO - tw * DWO) break;
                    __builtin_amdgcn_s_sleep(1);
                }
                u32x4 e[4];
#pragma unroll
                for (int i = 0; i < 4; i++) { e[i] = LDNT(rw, wof, (i & 1) * 4096 + (i >> 1) * 32768); SB(); }
#pragma unroll
                for (int i = 0; i < 4; i++) *(LAS u32x4*)(L + L_WO + tw * 4096 + i * 1024 + lane * 16) = e[i];
            }
        } else {
            const int kw = dw & 3;
            {
#pragma unroll 1
                for (u32 spin = 0; spin < SPIN_CAP; spin++) {
                    MEMB();
                    if (__ballot(*(LAS u32*)(L + L_FLG + 16 + (lane & 3) * 4) != tag) == 0) break;
                    __builtin_amdgcn_s_sleep(1);
                }
                asm volatile("s_setprio 1\n\t" KALL(>=)
                    : [r4] "+v"(rr[4]), [r5] "+v"(rr[5]), [r6] "+v"(rr[6]), [r7] "+v"(rr[7]),
                      [r8] "+v"(rr[8]), [r9] "+v"(rr[9]), [r10] "+v"(rr[10]), [r11] "+v"(rr[11]), [r12] "+v"(rr[12]), [r13] "+v"(rr[13]), [r14] "+v"(rr[14]), [r15] "+v"(rr[15])
                    : [ao0] "v"(ao), [ao1] "v"(ao1), [ao2] "v"(ao2), [ao3] "v"(ao3), [ra] "s"(ra), [s1] "s"(kz ^ 4096), [s2] "s"(kz ^ 8192), [s3] "s"(kz ^ 12288), [ks] "n"(KS) : "memory");
            }
            __builtin_amdgcn_s_setprio(3);
            u32x4 qx[4], qy[4];
            {
#pragma unroll 1
                for (u32 spin = 0; spin < 256; spin++) {
                    if ((u32)__builtin_amdgcn_s_memrealtime() - tstart >= TQ) break;
                    __builtin_amdgcn_s_sleep(1);
                }
                const int qof = WS_Q + (kvh * 4 + b) * 16384 + kw * 4096 + lane * 16;
                u32x2 g[4];
                asm volatile("" : "={v[112:115]}"(qy[0]), "={v[116:119]}"(qy[1]), "={v[120:123]}"(qy[2]), "={v[124:127]}"(qy[3]));
                poll<4>(qx, rs, qof, 1024);
                *(LAS u32*)(L + L_FLG + 80 + kw * 4) = tag;
                gather4<8, 4>(qx, qy, g, rs, qof, 1024, tag);
                const f32x2 qs = (__builtin_bit_cast(f32x2, g[0]) + __builtin_bit_cast(f32x2, g[1])) + (__builtin_bit_cast(f32x2, g[2]) + __builtin_bit_cast(f32x2, g[3]));
                *(LAS u32*)(L + L_Q + kw * 256 + lane * 4) = pack2(qs[0], qs[1]);
                LGK0();
                *(LAS u32*)(L + L_FLG + kw * 4) = tag;
#pragma unroll 1
                for (u32 spin = 0; spin < SPIN_CAP; spin++) {
                    MEMB();
                    const u32 f = *(LAS u32*)(L + L_FLG + (lane & 3) * 4);
                    if (__ballot(f != tag) == 0) break;
                }
            }
            MEMB();
            const int h = (n & 4) ? (~n & 3) : (n & 3);
            const u32x4 qf = *(LAS u32x4*)(L + L_Q + h * 256 + c * 64 + (n >> 2) * 16);
            const f32x4 z = {0.f, 0.f, 0.f, 0.f};
            const bool g0 = n & 4, g1 = n & 8;
            float sc[4];
#pragma unroll
            for (int e = 0; e < 2; e++) {
                float u[8][2], v[4];
#pragma unroll
                for (int j = 0; j < 8; j++) {
                    const f32x4 t = mfma16(rr[e * 8 + j], qf, z);
                    u[j][0] = g0 ? t[1] : t[0]; u[j][1] = g0 ? t[3] : t[2];
                }
#pragma unroll
                for (int j = 0; j < 4; j++) v[j] = (g1 ? u[j + 4][1] : u[j][0]) + dpp<ROR8>(g1 ? u[j][1] : u[j + 4][0]);
#pragma unroll
                for (int j = 0; j < 2; j++) {
                    const float d = (g0 ? v[j + 2] : v[j]) + dpp<HMIR>(g0 ? v[j] : v[j + 2]);
                    sc[e * 2 + j] = rbf(rbf(d) * 0.08838834764831845f) * LOG2E;
                }
            }
            float mx = fmaxf(fmaxf(sc[0], sc[1]), fmaxf(sc[2], sc[3]));
            mx = fmaxf(mx, dpp<HMIR>(mx)); mx = fmaxf(mx, dpp<ROR8>(mx));
            mx = x16_max(mx);
            float p[4];
#pragma unroll
            for (int j = 0; j < 4; j++) p[j] = __builtin_amdgcn_exp2f(sc[j] - mx);
            const u32 ppo = (kw * 512 + c * 64 + h * 16 + (n >> 2) * 4) ^ ((kw & 2) * 128);
            *(LAS u32*)(L + L_PP + ppo) = pack2(p[0], p[1]);
            *(LAS u32*)(L + L_PP + (ppo ^ 256)) = pack2(p[2], p[3]);
            float ls = (p[0] + p[1]) + (p[2] + p[3]);
            ls += dpp<HMIR>(ls); ls += dpp<ROR8>(ls);
            ls = x16_sum(ls);
            if (lane < 4) *(LAS f32x2*)(L + L_MX + n * 32 + kw * 8) = f32x2{mx, ls};
            LGK0();
            *(LAS u32*)(L + L_FLG + 48 + kw * 4) = tag;
            __builtin_amdgcn_s_setprio(1);
#pragma unroll 1
            for (u32 spin = 0; spin < SPIN_CAP; spin++) {
                MEMB();
                if (__ballot(*(LAS u32*)(L + L_FLG + 48 + (lane & 3) * 4) != tag) == 0) break;
            }
            MEMB();
            float fw[2];
            {
                const f32x2 ml = *(LAS f32x2*)(L + L_MX + n * 8);
                float M = fmaxf(ml[0], dpp<XOR1>(ml[0]));
                M = fmaxf(M, dpp<XOR2>(M));
                const float e = __builtin_amdgcn_exp2f(ml[0] - M);
                float ls = ml[1] * e;
                ls += dpp<XOR1>(ls); ls += dpp<XOR2>(ls);
                const float lse = M + __builtin_amdgcn_logf(ls);
                if (kw == 0) ST8((u32x2{__float_as_uint(lse), tag}), (lane & 51) == 0 ? WS_LSE + ((kvh * 4 + (lane >> 2)) * 32 + r) * 8 : OOB);
                const int f = __float_as_int(e * __builtin_amdgcn_rcpf(ls));
#pragma unroll
                for (int k = 0; k < 2; k++) fw[k] = __int_as_float(__builtin_amdgcn_ds_bpermute(((kw * 4 + (lane >> 5) * 2 + k) * 4), f));
            }
            {
                u32 pbo = L_PART + (lane >> 5) * 8192 + ((lane >> 2) & 1) * 1024 + ((lane >> 3) & 3) * 256 + ((lane & 3) * 4 + ((kw + 2 * ((lane >> 3) & 3) + ((lane >> 2) & 1)) & 3)) * 16;
                int oof = (lane & 33) ? OOB : WS_O + (kvh * 32 + r) * 1024 + kw * 256 + lane * 8;
                u32 pfa = L_FLG + 160 + min(n, 8) * 4;
                asm volatile("" : "+v"(pbo), "+v"(oof), "+v"(fw[0]), "+v"(fw[1]), "+v"(pfa));
                __builtin_amdgcn_s_setprio(3);
#pragma unroll 1
                for (u32 spin = 0; spin < SPIN_CAP; spin++) {
                    MEMB();
                    if (__ballot(*(LAS u32*)(L + pfa) != tag) == 0) break;
                    __builtin_amdgcn_s_sleep(1);
                }
                MEMB();
                LAS unsigned char* pb = L + pbo;
                f32x4 q0 = *(LAS f32x4*)(pb) + *(LAS f32x4*)(pb + 2048), q1 = *(LAS f32x4*)(pb + 4096) + *(LAS f32x4*)(pb + 6144);
                q0 = q0 * fw[0] + q1 * fw[1];
                const u32 pk = min(pack2(sw32(q0[0], q0[2]), sw32(q0[1], q0[3])), EMPTY - 1);
                auto t = __builtin_amdgcn_permlane32_swap(pk, pk, false, false);
                ST16((u32x4{t[0], t[1], (u32)__builtin_amdgcn_mov_dpp((int)t[0], XOR1, 0xf, 0xf, true), (u32)__builtin_amdgcn_mov_dpp((int)t[1], XOR1, 0xf, 0xf, true)}), oof);
                SB();
            }
            drain(qx, qy);
        }
        LGK0();
    }
}

extern "C" void kernel_launch(void* const* d_in, const int* in_sizes, int n_in,
                              void* d_out, int out_size, void* d_ws, size_t ws_size,
                              hipStream_t stream) {
    (void)in_sizes; (void)n_in; (void)out_size; (void)ws_size;
    (void)hipFuncSetAttribute((const void*)fused_kernel, hipFuncAttributeMaxDynamicSharedMemorySize, LDS_BYTES);
    fused_kernel<<<256, 1024, LDS_BYTES, stream>>>(
        (const u16*)d_in[1], (const u16*)d_in[2], (unsigned char*)d_ws, (const u16*)d_in[3],
        (const u16*)d_in[0], (const u16*)d_in[4],
        (const u16*)d_in[6], (const u16*)d_in[7], (const u16*)d_in[8], (const u16*)d_in[9],
        (u16*)d_out);
}
```
